```python
import torch

def _softmax(x, axis=-1):
    return torch.softmax(x, dim=axis)
def _gelu(x):
    return torch.nn.functional.gelu(x, approximate="tanh")
def _silu(x):
    return torch.nn.functional.silu(x)
def _sigmoid(x):
    return torch.sigmoid(x)
def _layernorm(x, axis=-1, eps=1e-5):
    m = x.mean(dim=axis, keepdim=True)
    v = x.var(dim=axis, keepdim=True, unbiased=False)
    return (x - m) / torch.sqrt(v + eps)
def _scatter_add(tgt, idx, src, axis=0):
    return tgt.clone().index_add_(axis, idx, src)
def _segment_sum(src, seg, n):
    out = torch.zeros((n,) + src.shape[1:], dtype=src.dtype)
    return out.index_add_(0, seg, src)
def _pad(x, pw):
    flat = tuple(v for pair in reversed(pw) for v in pair)
    return torch.nn.functional.pad(x, flat)

kernel_name = 'parallel_sum__mha_block_cached__glu_gated_glu__decode'

def setup_inputs():

    _w = (lambda a: (lambda t: t.to(torch.bfloat16) if t.is_floating_point() else t)(torch.as_tensor(a)))
    def _A_setup():
        B, Sq, D = 1, 1, 640
        H, Sk, Dh = 32, 4096, 192
        return {
            "x": _w((lambda *s: torch.randn(*s, dtype=torch.bfloat16) * 0.05)(B, Sq, D)),
            "k_cache": _w((lambda *s: torch.randn(*s, dtype=torch.bfloat16) * 3.2)(B, H, Sk, Dh)),
            "v_cache": _w((lambda *s: torch.randn(*s, dtype=torch.bfloat16) * 1.6)(B, H, Sk, Dh)),
            "wq": _w((lambda *s: torch.randn(*s, dtype=torch.bfloat16) * 3.2)(D, H * Dh) * (D ** -0.5)),
            "wo": _w((lambda *s: torch.randn(*s, dtype=torch.bfloat16) * 1.6)(H * Dh, D) * ((H * Dh) ** -0.5)),
        }
    _A_in = _A_setup()

    def _B_setup():
        B, S, D, F = 1, 1, 640, 1536
        return {
            "x": _w((lambda *s: torch.randn(*s, dtype=torch.bfloat16) * 0.05)(B, S, D)),
            "w_gate": _w((lambda *s: torch.randn(*s, dtype=torch.bfloat16) * 1.6)(D, F)),
            "w_up": _w((lambda *s: torch.randn(*s, dtype=torch.bfloat16) * 0.1)(D, F)),
            "w_down": _w((lambda *s: torch.randn(*s, dtype=torch.bfloat16) * 0.1)(F, D)),
        }
    _B_in = _B_setup()

    return {
        "A_x": _A_in["x"],
        "A_k_cache": _A_in["k_cache"],
        "A_v_cache": _A_in["v_cache"],
        "A_wq": _A_in["wq"],
        "A_wo": _A_in["wo"],
        "B_x": _B_in["x"],
        "B_w_gate": _B_in["w_gate"],
        "B_w_up": _B_in["w_up"],
        "B_w_down": _B_in["w_down"],
    }

def reference(A_x, A_k_cache, A_v_cache, A_wq, A_wo, B_x, B_w_gate, B_w_up, B_w_down):
    def _A_ref(x, k_cache, v_cache, wq, wo):
        B, Sq, D = x.shape
        H, Dh = 32, 192
        q = (lambda x, *d: x.permute(*d))((x @ wq).reshape(B, Sq, H, Dh), 0, 2, 1, 3)
        scores = torch.einsum("bhqd,bhkd->bhqk", q, k_cache) * (Dh ** -0.5)
        p = _softmax(scores, axis=-1)
        o = torch.einsum("bhqk,bhkd->bhqd", p, v_cache)
        return (lambda x, *d: x.permute(*d))(o, 0, 2, 1, 3).reshape(B, Sq, H * Dh) @ wo

    def _B_ref(x, w_gate, w_up, w_down):
        g = torch.einsum("bsd,df->bsf", x, w_gate)
        u = torch.einsum("bsd,df->bsf", x, w_up)
        h = _sigmoid(g) * u
        return torch.einsum("bsf,fd->bsd", h, w_down)

    x = A_x
    xn = _layernorm(x, axis=-1)
    return x + _A_ref(xn, A_k_cache, A_v_cache, A_wq, A_wo) + _B_ref(xn, B_w_gate, B_w_up, B_w_down)
```

```cpp
#include <hip/hip_runtime.h>
#include <hip/hip_bf16.h>
#define MIMIC 1
typedef unsigned short u16;
typedef unsigned u32x4 __attribute__((ext_vector_type(4)));
constexpr int D = 640, H = 32, SK = 4096, DH = 192, F = 1536, HD = H * DH;
constexpr int NSPLIT = 16, KPS = SK / NSPLIT;
__device__ __forceinline__ float bf2f(u16 b) { return __uint_as_float((unsigned)b << 16); }
__device__ __forceinline__ float rbf(float f) {
  unsigned u = __float_as_uint(f); u += 0x7FFFu + ((u >> 16) & 1u); return __uint_as_float(u & 0xFFFF0000u);
}
__device__ __forceinline__ u16 f2bf(float f) { unsigned u = __float_as_uint(f); u += 0x7FFFu + ((u >> 16) & 1u); return (u16)(u >> 16); }

__device__ void layernorm_lds(const u16* x, float* xn, float* red) {
  int t = threadIdx.x;
  float s = 0;
  for (int i = t; i < D; i += blockDim.x) s += bf2f(x[i]);
  red[t] = s; __syncthreads();
  for (int o = blockDim.x / 2; o > 0; o >>= 1) { if (t < o) red[t] += red[t + o]; __syncthreads(); }
  float mean = red[0] / D; __syncthreads();
  s = 0;
  for (int i = t; i < D; i += blockDim.x) { float d = bf2f(x[i]) - mean; s += d * d; }
  red[t] = s; __syncthreads();
  for (int o = blockDim.x / 2; o > 0; o >>= 1) { if (t < o) red[t] += red[t + o]; __syncthreads(); }
  float var = red[0] / D; __syncthreads();
  float m = rbf(mean), v = rbf(var);
  float den = rbf(sqrtf(rbf(v + 1e-5f)));
  for (int i = t; i < D; i += blockDim.x) xn[i] = rbf(rbf(bf2f(x[i]) - m) / den);
  __syncthreads();
}

__global__ void __launch_bounds__(256) k1(const u16* x, const u16* wq, const u16* wg, const u16* wu, float* q, float* hbuf) {
  __shared__ float xn[D]; __shared__ float red[256]; __shared__ float part[2][4][64];
  layernorm_lds(x, xn, red);
  int t = threadIdx.x, c = t & 63, r = t >> 6;
  int b = blockIdx.x;
  if (b < HD / 64) {
    int col = b * 64 + c; float a = 0;
    for (int k = r; k < D; k += 4) a += xn[k] * bf2f(wq[(size_t)k * HD + col]);
    part[0][r][c] = a; __syncthreads();
    if (r == 0) q[col] = rbf(part[0][0][c] + part[0][1][c] + part[0][2][c] + part[0][3][c]);
  } else {
    int col = (b - HD / 64) * 64 + c; float a = 0, u = 0;
    for (int k = r; k < D; k += 4) { a += xn[k] * bf2f(wg[(size_t)k * F + col]); u += xn[k] * bf2f(wu[(size_t)k * F + col]); }
    part[0][r][c] = a; part[1][r][c] = u; __syncthreads();
    if (r == 0) {
      float g = rbf(part[0][0][c] + part[0][1][c] + part[0][2][c] + part[0][3][c]);
      float uu = rbf(part[1][0][c] + part[1][1][c] + part[1][2][c] + part[1][3][c]);
      float sg = rbf(1.f / (1.f + __expf(-g)));
      hbuf[col] = rbf(sg * uu);
    }
  }
}
__global__ void __launch_bounds__(256) k2(const u16* K, const float* q, float* sc) {
  int h = blockIdx.y, s = blockIdx.x, t = threadIdx.x;
  __shared__ float qs[DH];
  if (t < DH) qs[t] = q[h * DH + t];
  __syncthreads();
  int key = s * KPS + t;
  const u32x4* kp = (const u32x4*)(K + ((size_t)h * SK + key) * DH);
  float a = 0;
  for (int i = 0; i < DH / 8; ++i) { u32x4 v = kp[i];
    for (int j = 0; j < 4; ++j) { a += qs[i * 8 + 2 * j] * __uint_as_float(v[j] << 16); a += qs[i * 8 + 2 * j + 1] * __uint_as_float(v[j] & 0xFFFF0000u); } }
  sc[h * SK + key] = rbf(rbf(a) * 0.07216878364870322f);
}
__global__ void __launch_bounds__(256) k3(const u16* V, const float* sc, float* opart) {
  int h = blockIdx.y, s = blockIdx.x, t = threadIdx.x;
  __shared__ float red[256]; __shared__ float p[KPS];
  float m = -INFINITY;
  for (int i = t; i < SK; i += 256) m = fmaxf(m, sc[h * SK + i]);
  red[t] = m; __syncthreads();
  for (int o = 128; o > 0; o >>= 1) { if (t < o) red[t] = fmaxf(red[t], red[t + o]); __syncthreads(); }
  m = red[0]; __syncthreads();
  float l = 0;
  for (int i = t; i < SK; i += 256) l += __expf(sc[h * SK + i] - m);
  red[t] = l; __syncthreads();
  for (int o = 128; o > 0; o >>= 1) { if (t < o) red[t] += red[t + o]; __syncthreads(); }
  l = red[0]; __syncthreads();
  p[t] = rbf(__expf(sc[h * SK + s * KPS + t] - m) / l);
  __syncthreads();
  if (t < DH) {
    float a = 0;
    for (int k = 0; k < KPS; ++k) a += p[k] * bf2f(V[((size_t)h * SK + s * KPS + k) * DH + t]);
    opart[(h * NSPLIT + s) * DH + t] = a;
  }
}
__global__ void __launch_bounds__(640) k4(const float* opart, const float* hbuf, const u16* wo, const u16* wd, float* ypart) {
  int b = blockIdx.x, t = threadIdx.x;
  __shared__ float v[DH];
  if (b < H) {
    if (t < DH) { float a = 0; for (int s = 0; s < NSPLIT; ++s) a += opart[(b * NSPLIT + s) * DH + t]; v[t] = rbf(a); }
    __syncthreads();
    float a = 0; for (int k = 0; k < DH; ++k) a += v[k] * bf2f(wo[((size_t)b * DH + k) * D + t]);
    ypart[b * D + t] = a;
  } else {
    int j = b - H;
    if (t < DH) v[t] = hbuf[j * DH + t];
    __syncthreads();
    float a = 0; for (int k = 0; k < DH; ++k) a += v[k] * bf2f(wd[((size_t)j * DH + k) * D + t]);
    ypart[b * D + t] = a;
  }
}
__global__ void __launch_bounds__(640) k5(const float* ypart, const u16* x, u16* out) {
  int t = threadIdx.x;
  float a = 0, bb = 0;
  for (int b = 0; b < H; ++b) a += ypart[b * D + t];
  for (int b = H; b < H + F / DH; ++b) bb += ypart[b * D + t];
  float r = rbf(bf2f(x[t]) + rbf(a));
  out[t] = f2bf(r + rbf(bb));
}

extern "C" void kernel_launch(void* const* d_in, const int* in_sizes, int n_in, void* d_out, int out_size, void* d_ws, size_t ws_size, hipStream_t stream) {
  const u16* x = (const u16*)d_in[0]; const u16* K = (const u16*)d_in[1]; const u16* V = (const u16*)d_in[2];
  const u16* wq = (const u16*)d_in[3]; const u16* wo = (const u16*)d_in[4];
  const u16* wg = (const u16*)d_in[6]; const u16* wu = (const u16*)d_in[7]; const u16* wd = (const u16*)d_in[8];
  float* ws = (float*)d_ws;
  float* q = ws; float* hbuf = ws + 8192; float* sc = ws + 16384; float* opart = sc + H * SK; float* ypart = opart + H * NSPLIT * DH;
  k1<<<HD / 64 + F / 64, 256, 0, stream>>>(x, wq, wg, wu, q, hbuf);
  k2<<<dim3(NSPLIT, H), 256, 0, stream>>>(K, q, sc);
  k3<<<dim3(NSPLIT, H), 256, 0, stream>>>(V, sc, opart);
  k4<<<H + F / DH, 640, 0, stream>>>(opart, hbuf, wo, wd, ypart);
  k5<<<1, 640, 0, stream>>>(ypart, x, (u16*)d_out);
}
```

```cpp
#include <hip/hip_runtime.h>
#include <hip/hip_bf16.h>

#define KV_AUX 2
#define W_AUX 2
#define WIN 8
#define HPRIO 1
#define ABL_WO 0
#define ABL_POLL 0
#define ABL_HELP 0
#define ABL_ST 0
#define STAMP 0

typedef unsigned u32;
typedef unsigned short u16;
typedef unsigned long long u64;
typedef u32 v2u __attribute__((ext_vector_type(2)));
typedef u32 v3u __attribute__((ext_vector_type(3)));
typedef u32 v4u __attribute__((ext_vector_type(4)));
typedef __bf16 bf16x2 __attribute__((ext_vector_type(2)));
#define LAS __attribute__((address_space(3)))
typedef __amdgpu_buffer_rsrc_t rsrc_t;

constexpr int D = 640, NH = 32, SK = 4096, DH = 192, FF = 1536, HD = NH * DH;
constexpr int NSTREAM = 8, NHELP = 4, NTHR = 64 * (NSTREAM + NHELP);
constexpr int SC1 = 16;
constexpr int OFF_E = 0;
constexpr int OFF_G0 = 4096, G0_STR = 2304;
constexpr int OFF_G1 = OFF_G0 + 256 * G0_STR, G1_STR = 1664;
constexpr int OFF_GA = OFF_G1 + 256 * G1_STR;
constexpr int OFF_GB = OFF_GA + 256 * 640;
constexpr int WS_END = OFF_GB + 256 * 640;
constexpr int OFF_STAMP = 2 << 20;
constexpr int L_CNT = 0, L_HQ = 16, L_HG = L_HQ + 4 * 192, L_QS = L_HG + 4 * 48, L_ACC = L_QS + 96, L_ML = L_ACC + 8 * 192,
              L_Y = L_ML + 16, L_END = L_Y + 8 * 80;
constexpr u32 SPIN_G = 1u << 17, SPIN_L = 1u << 21;

struct Params {
  const u16 *x, *k, *v, *wq, *wo, *wg, *wu, *wd;
  u16* out;
  unsigned char* ws;
};

__device__ __forceinline__ float f_(u32 u) { return __builtin_bit_cast(float, u); }
__device__ __forceinline__ u32 u_(float f) { return __builtin_bit_cast(u32, f); }
__device__ __forceinline__ float dot2(u32 a, u32 b, float c) {
  return __builtin_amdgcn_fdot2_f32_bf16(__builtin_bit_cast(bf16x2, a), __builtin_bit_cast(bf16x2, b), c, false);
}
__device__ __forceinline__ u32 bfbits(float f) { return (u32)__builtin_bit_cast(u16, (__bf16)f); }
__device__ __forceinline__ float rbf(float f) { return f_(bfbits(f) << 16); }
template <int CTRL> __device__ __forceinline__ float dpp(float x) {
  return f_((u32)__builtin_amdgcn_mov_dpp((int)u_(x), CTRL, 0xf, 0xf, true));
}
constexpr int X1 = 0xB1, X2 = 0x4E, X7 = 0x141, X8 = 0x128;
__device__ __forceinline__ float mrg16(float a, float b) {
  auto r = __builtin_amdgcn_permlane16_swap(u_(a), u_(b), false, false);
  return f_(r[0]) + f_(r[1]);
}
__device__ __forceinline__ float mrg32(float a, float b) {
  auto r = __builtin_amdgcn_permlane32_swap(u_(a), u_(b), false, false);
  return f_(r[0]) + f_(r[1]);
}
template <int CTRL> __device__ __forceinline__ float mrgd(float a, float b, bool bit) {
  const float ta = a + dpp<CTRL>(a), tb = b + dpp<CTRL>(b);
  return bit ? tb : ta;
}
__device__ __forceinline__ float sum8(float x) { x += dpp<X1>(x); x += dpp<X2>(x); x += dpp<X7>(x); return x; }
__device__ __forceinline__ float max8(float x) { x = fmaxf(x, dpp<X1>(x)); x = fmaxf(x, dpp<X2>(x)); x = fmaxf(x, dpp<X7>(x)); return x; }
__device__ __forceinline__ float sumhi(float x) { x += dpp<X8>(x); x = mrg16(x, x); return mrg32(x, x); }
__device__ __forceinline__ float maxhi(float x) {
  x = fmaxf(x, dpp<X8>(x));
  auto r = __builtin_amdgcn_permlane16_swap(u_(x), u_(x), false, false); x = fmaxf(f_(r[0]), f_(r[1]));
  auto t = __builtin_amdgcn_permlane32_swap(u_(x), u_(x), false, false); return fmaxf(f_(t[0]), f_(t[1]));
}
__device__ __forceinline__ float sum32(float x) { x = sum8(x); x += dpp<X8>(x); return mrg16(x, x); }
__device__ __forceinline__ float sum64(float x) { x = sum32(x); return mrg32(x, x); }
__device__ __forceinline__ float max64(float x) {
  x = max8(x); x = fmaxf(x, dpp<X8>(x));
  auto r = __builtin_amdgcn_permlane16_swap(u_(x), u_(x), false, false); x = fmaxf(f_(r[0]), f_(r[1]));
  auto t = __builtin_amdgcn_permlane32_swap(u_(x), u_(x), false, false); return fmaxf(f_(t[0]), f_(t[1]));
}
__device__ __forceinline__ rsrc_t mk(const void* p, int bytes) { return __builtin_amdgcn_make_buffer_rsrc((void*)p, 0, bytes, 0x00020000); }
#define LGKM0() asm volatile("s_waitcnt lgkmcnt(0)" ::: "memory")
#define BAR() do { asm volatile("s_waitcnt lgkmcnt(0)\n\ts_barrier" ::: "memory"); } while (0)
#define PACE(n) asm volatile("s_waitcnt vmcnt(%0)" ::"i"(n))
#define SB() __builtin_amdgcn_sched_barrier(0)
#define NAP() asm volatile("s_sleep 1" ::: "memory")
constexpr int brev5(int n) { return ((n & 1) << 4) | ((n & 2) << 2) | (n & 4) | ((n & 8) >> 2) | ((n & 16) >> 4); }

template <int N> struct IC { static constexpr int v = N; };
template <int I, int N, class Fn> __device__ __forceinline__ void sfor(Fn&& f) {
  if constexpr (I < N) { f(IC<I>{}); sfor<I + 1, N>(f); }
}
__device__ __forceinline__ void lds_wait_ge(volatile LAS u32* p, u32 want) {
#pragma unroll 1
  for (u32 i = 0; i < SPIN_L; ++i) { if (*p >= want) break; NAP(); }
  asm volatile("" ::: "memory");
}
__device__ __forceinline__ void lds_signal(LAS u32* p, int lane) {
  LGKM0();
  if (lane == 0) __hip_atomic_fetch_add(p, 1u, __ATOMIC_RELAXED, __HIP_MEMORY_SCOPE_WORKGROUP);
}
__device__ __forceinline__ void axpy10(float (&y)[10], v4u a, u32 b, u32 hb) {
  const u32 hh = hb << 16;
  y[0] = dot2(a.x, hb, y[0]); y[1] = dot2(a.x, hh, y[1]); y[2] = dot2(a.y, hb, y[2]); y[3] = dot2(a.y, hh, y[3]);
  y[4] = dot2(a.z, hb, y[4]); y[5] = dot2(a.z, hh, y[5]); y[6] = dot2(a.w, hb, y[6]); y[7] = dot2(a.w, hh, y[7]);
  y[8] = dot2(b, hb, y[8]); y[9] = dot2(b, hh, y[9]);
}
__device__ __forceinline__ void axpy6(float (&y)[6], v3u a, u32 hb) {
  const u32 hh = hb << 16;
  y[0] = dot2(a.x, hb, y[0]); y[1] = dot2(a.x, hh, y[1]); y[2] = dot2(a.y, hb, y[2]); y[3] = dot2(a.y, hh, y[3]);
  y[4] = dot2(a.z, hb, y[4]); y[5] = dot2(a.z, hh, y[5]);
}
__device__ __forceinline__ void red10(const float (&y)[10], int lane, float& v0, float& v1) {
  const float z0 = mrg32(y[0], y[1]), z1 = mrg32(y[2], y[3]), z2 = mrg32(y[4], y[5]), z3 = mrg32(y[6], y[7]), z4 = mrg32(y[8], y[9]);
  const float u0 = mrg16(z0, z1), u1 = mrg16(z2, z3), u2 = mrg16(z4, z4);
  v0 = mrgd<X8>(u0, u1, (lane & 8) != 0);
  v1 = u2 + dpp<X8>(u2);
}
#define TS(i)
#define TSW(i)

__global__ void __launch_bounds__(NTHR, 3) fused_decode(Params p) {
  __shared__ __attribute__((aligned(16))) u32 lds_[L_END];
  LAS u32* L = (LAS u32*)lds_;
  LAS float* Lf = (LAS float*)lds_;
  const int tid = threadIdx.x, lane = tid & 63, wave = __builtin_amdgcn_readfirstlane(tid >> 6);
  const int b = blockIdx.x, h = (b & 7) * 4 + (b >> 6), s = (b >> 3) & 7, g = h * 8 + s;
  const int hf = lane >> 5, c = lane & 31, pg = lane >> 3, s8 = lane & 7;
  const rsrc_t rws = mk(p.ws, WS_END);
  TS(0);
  asm volatile("" ::"s"(p.x), "s"(p.k), "s"(p.v), "s"(p.wq), "s"(p.wo), "s"(p.wg), "s"(p.wu), "s"(p.wd), "s"(p.out), "s"(p.ws));
  u32 eraw = __builtin_amdgcn_raw_buffer_load_b32(rws, OFF_E, 0, SC1);
  const int col0 = 8 * s8 + ((lane >> 1) & 4) + ((lane >> 3) & 2) + hf, col1 = 64 + 2 * s8 + hf;

  if (wave >= NSTREAM) {
    const int hw = wave - NSTREAM;
    __builtin_amdgcn_s_setprio(3);
    const rsrc_t rx = mk(p.x, D * 2);
    const v4u xa = __builtin_amdgcn_raw_buffer_load_b128(rx, lane * 16, 0, 0);
    const u32 xb = __builtin_amdgcn_raw_buffer_load_b32(rx, 1024 + lane * 4, 0, 0);
    const int myrow = 80 * s + (lane < 20 ? 20 * hw + lane : 40 * (hw & 1) + min(lane - 20, 39));
    const u32 xr = __builtin_amdgcn_raw_buffer_load_b16(rx, myrow * 2, 0, 0);
    const rsrc_t rq = mk(p.wq, (ABL_HELP & 2) ? 0 : D * HD * 2);
    const rsrc_t rg = mk(hw < 2 ? p.wg : p.wu, (ABL_HELP & 1) ? 0 : D * FF * 2);
    v3u wqv[10], guv[5];
    {
      const int o = (80 * s + 20 * hw + hf) * (HD * 2) + h * (DH * 2) + c * 12;
#pragma unroll
      for (int r = 0; r < 10; ++r) wqv[r] = __builtin_amdgcn_raw_buffer_load_b96(rq, o, r * 2 * HD * 2, W_AUX);
      const int o2 = (80 * s + 40 * (hw & 1) + pg) * (FF * 2) + h * 96 + s8 * 12;
#pragma unroll
      for (int r = 0; r < 5; ++r) guv[r] = __builtin_amdgcn_raw_buffer_load_b96(rg, o2, r * 8 * FF * 2, W_AUX);
    }
    if (hw == 0 && lane < 16) L[L_CNT + lane] = 0u;
    SB();
    BAR();
    SB();
    float xv[10];
    xv[0] = f_(xa.x << 16); xv[1] = f_(xa.x & 0xffff0000u); xv[2] = f_(xa.y << 16); xv[3] = f_(xa.y & 0xffff0000u);
    xv[4] = f_(xa.z << 16); xv[5] = f_(xa.z & 0xffff0000u); xv[6] = f_(xa.w << 16); xv[7] = f_(xa.w & 0xffff0000u);
    xv[8] = f_(xb << 16); xv[9] = f_(xb & 0xffff0000u);
    float sm = 0.f;
#pragma unroll
    for (int i = 0; i < 10; ++i) sm += xv[i];
    const float mean = sum64(sm) * (1.0f / D);
    float sq = 0.f;
#pragma unroll
    for (int i = 0; i < 10; ++i) { const float d = xv[i] - mean; sq = fmaf(d, d, sq); }
    const float var = sum64(sq) * (1.0f / D);
    const float den = rbf(sqrtf(rbf(rbf(var) + 1e-5f)));
    const u32 xn = bfbits(rbf(f_(xr << 16) - rbf(mean)) / den);
    float qa[6] = {0.f, 0.f, 0.f, 0.f, 0.f, 0.f}, ga[6] = {0.f, 0.f, 0.f, 0.f, 0.f, 0.f};
#pragma unroll
    for (int r = 0; r < 10; ++r) axpy6(qa, wqv[r], (u32)__builtin_amdgcn_ds_bpermute((2 * r + hf) * 4, (int)xn));
#pragma unroll
    for (int r = 0; r < 5; ++r) axpy6(ga, guv[r], (u32)__builtin_amdgcn_ds_bpermute((20 + 8 * r + pg) * 4, (int)xn));
#pragma unroll
    for (int i = 0; i < 3; ++i) {
      Lf[L_HQ + hw * 192 + 6 * c + 2 * i + hf] = mrg32(qa[2 * i], qa[2 * i + 1]);
      float t = mrg32(ga[2 * i], ga[2 * i + 1]);
      t = mrg16(t, t); t += dpp<X8>(t);
      if ((pg & 3) == 0) Lf[L_HG + hw * 48 + 6 * s8 + 2 * i + hf] = t;
    }
    asm volatile("" : "+v"(eraw));
    const u32 tag = eraw + 1u;
    TS(10);
    lds_signal(L + L_CNT + 0, lane);
    lds_wait_ge(L + L_CNT + 0, 4u);
    TS(11);
    if (hw < 3) {
      const int col = hw * 64 + lane;
      const float v = (Lf[L_HQ + col] + Lf[L_HQ + 192 + col]) + (Lf[L_HQ + 384 + col] + Lf[L_HQ + 576 + col]);
      __builtin_amdgcn_raw_buffer_store_b64(v2u{u_(v), tag}, rws, OFF_G0 + g * G0_STR + col * 8, 0, SC1);
      TS(1);
      const int o = OFF_G0 + (h * 8 + pg) * G0_STR + (64 * hw + 2 * s8) * 8;
      v4u q4[4];
      bool ok = false;
#pragma unroll 1
      for (u32 it = 0; it < SPIN_G; ++it) {
        if (!ok) {
#pragma unroll
          for (int i = 0; i < 4; ++i) q4[i] = __builtin_amdgcn_raw_buffer_load_b128(rws, o, i * 128, SC1);
          ok = true;
#pragma unroll
          for (int i = 0; i < 4; ++i) ok &= (q4[i].y == tag) & (q4[i].w == tag);
        }
        if (ABL_POLL || __all(ok)) break;
        NAP();
      }
#pragma unroll
      for (int i = 0; i < 4; ++i) {
        const u32 pk = bfbits(sumhi(f_(q4[i].x))) | (bfbits(sumhi(f_(q4[i].z))) << 16);
        if (pg == 0) L[L_QS + 32 * hw + 8 * i + s8] = pk;
      }
      lds_signal(L + L_CNT + 1, lane);
      TS(2);
      if (hw == 0) { TSW(1); TSW(2); TSW(10); TSW(11); }
    } else {
      if (lane < 48) {
        const float gv = Lf[L_HG + lane] + Lf[L_HG + 48 + lane], uv = Lf[L_HG + 96 + lane] + Lf[L_HG + 144 + lane];
        __builtin_amdgcn_raw_buffer_store_b128(v4u{u_(gv), tag, u_(uv), tag}, rws, OFF_G0 + g * G0_STR + 1536 + lane * 16, 0, SC1);
      }
      const rsrc_t rd = mk(p.wd, (ABL_HELP & 1) ? 0 : FF * D * 2);
      v4u da[6]; u32 db[6];
      const int od = (48 * h + pg) * (D * 2) + s * 160;
#pragma unroll
      for (int i = 0; i < 6; ++i) {
        da[i] = __builtin_amdgcn_raw_buffer_load_b128(rd, od + 16 * s8, i * 8 * D * 2, W_AUX);
        db[i] = __builtin_amdgcn_raw_buffer_load_b32(rd, od + 128 + 4 * s8, i * 8 * D * 2, W_AUX);
      }
      SB();
      const int o = OFF_G0 + (h * 8 + pg) * G0_STR + 1536 + s8 * 16;
      v4u g4[6];
      bool ok = false;
#pragma unroll 1
      for (u32 it = 0; it < SPIN_G; ++it) {
        if (!ok) {
#pragma unroll
          for (int i = 0; i < 6; ++i) g4[i] = __builtin_amdgcn_raw_buffer_load_b128(rws, o, i * 128, SC1);
          ok = true;
#pragma unroll
          for (int i = 0; i < 6; ++i) ok &= (g4[i].y == tag) & (g4[i].w == tag);
        }
        if (ABL_POLL || __all(ok)) break;
        NAP();
      }
      float y[10] = {0.f, 0.f, 0.f, 0.f, 0.f, 0.f, 0.f, 0.f, 0.f, 0.f};
#pragma unroll
      for (int i = 0; i < 6; ++i) {
        const float gg = rbf(sumhi(f_(g4[i].x))), uu = rbf(sumhi(f_(g4[i].z)));
        const float sg = rbf(1.0f / (1.0f + __expf(-gg)));
        axpy10(y, da[i], db[i], (u32)__builtin_amdgcn_ds_bpermute(pg * 4, (int)bfbits(sg * uu)));
      }
      float v0, v1;
      red10(y, lane, v0, v1);
      const int ob = OFF_GB + (s * 32 + h) * 640;
      __builtin_amdgcn_raw_buffer_store_b64(v2u{u_(v0), tag}, rws, ob + col0 * 8, 0, SC1);
      if ((lane & 24) == 0) __builtin_amdgcn_raw_buffer_store_b64(v2u{u_(v1), tag}, rws, ob + col1 * 8, 0, SC1);
      TS(9); TSW(9);
    }
    return;
  }

  const size_t kvo = ((size_t)h * SK + 512 * s) * (DH * 2);
  const rsrc_t rk = mk((const char*)p.k + kvo, 512 * DH * 2), rv = mk((const char*)p.v + kvo, 512 * DH * 2);
  const int vo = wave * (64 * DH * 2) + lane * 12;
  SB();
  BAR();
  SB();
  v3u kv[32];
#pragma unroll
  for (int n = 0; n < 32; ++n) {
    if (n >= WIN) PACE(WIN - 1);
    kv[n] = __builtin_amdgcn_raw_buffer_load_b96(rk, vo, n * 768, KV_AUX);
    SB();
  }
  TS(3);
  lds_wait_ge(L + L_CNT + 1, 3u);
  TS(4);
  const u32 q0 = L[L_QS + 3 * c], q1 = L[L_QS + 3 * c + 1], q2 = L[L_QS + 3 * c + 2];
  float t1[16];
  constexpr int CPS = 32 / WIN;
  static_assert(CPS * WIN == 32 && CPS % 2 == 0, "window");
#pragma unroll
  for (int n = 0; n < WIN; ++n) {
#pragma unroll
    for (int a = n * CPS / 2; a < (n + 1) * CPS / 2; ++a) {
      const float s0 = dot2(kv[2 * a].z, q2, dot2(kv[2 * a].y, q1, dot2(kv[2 * a].x, q0, 0.f)));
      const float s1 = dot2(kv[2 * a + 1].z, q2, dot2(kv[2 * a + 1].y, q1, dot2(kv[2 * a + 1].x, q0, 0.f)));
      t1[a] = mrg16(s0, s1);
    }
    SB();
    PACE(WIN - 1);
    kv[n] = __builtin_amdgcn_raw_buffer_load_b96(rv, vo, n * 768, KV_AUX);
    SB();
  }
  kv[WIN] = __builtin_amdgcn_raw_buffer_load_b96(rv, vo, WIN * 768, KV_AUX);
  SB();
  float t2[8], t3[4];
#pragma unroll
  for (int a = 0; a < 8; ++a) t2[a] = mrgd<X8>(t1[2 * a], t1[2 * a + 1], (lane & 8) != 0);
#pragma unroll
  for (int a = 0; a < 4; ++a) t3[a] = mrgd<X7>(t2[2 * a], t2[2 * a + 1], (lane & 4) != 0);
  const float t40 = mrgd<X2>(t3[0], t3[1], (lane & 2) != 0), t41 = mrgd<X2>(t3[2], t3[3], (lane & 2) != 0);
  const float sc = rbf(rbf(mrgd<X1>(t40, t41, (lane & 1) != 0)) * 0.07216878364870322f);
  const float mw = max64(sc);
  const float e = __expf(sc - mw);
  const float lw = sum64(e);
  const u32 pb = bfbits(e);
  if (lane == 0) { Lf[L_ML + 2 * wave] = mw; Lf[L_ML + 2 * wave + 1] = lw; }
  const rsrc_t ro = mk(p.wo, ABL_WO ? 0 : HD * D * 2);
  const int oo = (DH * h + 24 * wave + pg) * (D * 2) + s * 160;
  v4u wa[3]; u32 wb[3];
  float acc[6] = {0.f, 0.f, 0.f, 0.f, 0.f, 0.f};
  sfor<0, 32>([&](auto ic) {
    constexpr int n = decltype(ic)::v;
    const u32 pn = (u32)__builtin_amdgcn_ds_swizzle((int)pb, brev5(n) << 5);
    axpy6(acc, kv[n], pn);
    SB();
    constexpr int m = n + WIN + 1;
    if constexpr (m < 32) kv[m] = __builtin_amdgcn_raw_buffer_load_b96(rv, vo, m * 768, KV_AUX);
    else if constexpr (m < 35) {
      wa[m - 32] = __builtin_amdgcn_raw_buffer_load_b128(ro, oo + 16 * s8, (m - 32) * 8 * D * 2, W_AUX);
      wb[m - 32] = __builtin_amdgcn_raw_buffer_load_b32(ro, oo + 128 + 4 * s8, (m - 32) * 8 * D * 2, W_AUX);
    }
    SB();
  });
#pragma unroll
  for (int i = 0; i < 3; ++i) Lf[L_ACC + wave * 192 + 6 * c + 2 * i + hf] = mrg32(acc[2 * i], acc[2 * i + 1]);
  asm volatile("" : "+v"(eraw));
  const u32 tag = eraw + 1u;
  BAR();
  TS(5);
  if (wave < 4) {
    float mm[8], M = -INFINITY;
#pragma unroll
    for (int w = 0; w < 8; ++w) { mm[w] = Lf[L_ML + 2 * w]; M = fmaxf(M, mm[w]); }
    float o = 0.f;
    if (wave < 3) {
#pragma unroll
      for (int w = 0; w < 8; ++w) o = fmaf(__expf(mm[w] - M), Lf[L_ACC + w * 192 + tid], o);
      __builtin_amdgcn_raw_buffer_store_b64(v2u{u_(o), tag}, rws, OFF_G1 + g * G1_STR + tid * 8, 0, SC1);
    } else {
#pragma unroll
      for (int w = 0; w < 8; ++w) o = fmaf(__expf(mm[w] - M), Lf[L_ML + 2 * w + 1], o);
      if (lane == 0) __builtin_amdgcn_raw_buffer_store_b128(v4u{u_(M), tag, u_(o), tag}, rws, OFF_G1 + g * G1_STR + 1536, 0, SC1);
    }
  }
  {
    const int o1 = OFF_G1 + (h * 8 + pg) * G1_STR;
    v2u og[3]; v4u ml;
    bool ok = false;
#pragma unroll 1
    for (u32 it = 0; it < SPIN_G; ++it) {
      if (!ok) {
#pragma unroll
        for (int i = 0; i < 3; ++i) og[i] = __builtin_amdgcn_raw_buffer_load_b64(rws, o1 + (24 * wave + s8) * 8, i * 64, SC1);
        ml = __builtin_amdgcn_raw_buffer_load_b128(rws, o1 + 1536, 0, SC1);
        ok = (og[0].y == tag) & (og[1].y == tag) & (og[2].y == tag) & (ml.y == tag) & (ml.w == tag);
      }
      if (__all(ok)) break;
      NAP();
    }
    TS(6);
    const float M = maxhi(f_(ml.x));
    const float wgt = __expf(f_(ml.x) - M);
    const float inv = 1.0f / sumhi(wgt * f_(ml.z));
    float y[10] = {0.f, 0.f, 0.f, 0.f, 0.f, 0.f, 0.f, 0.f, 0.f, 0.f};
#pragma unroll
    for (int i = 0; i < 3; ++i)
      axpy10(y, wa[i], wb[i], (u32)__builtin_amdgcn_ds_bpermute(pg * 4, (int)bfbits(sumhi(wgt * f_(og[i].x)) * inv)));
    float v0, v1;
    red10(y, lane, v0, v1);
    Lf[L_Y + wave * 80 + col0] = v0;
    if ((lane & 24) == 0) Lf[L_Y + wave * 80 + col1] = v1;
    BAR();
    TS(7);
    if (wave == 0) { TSW(0); TSW(3); TSW(4); TSW(5); TSW(6); TSW(7); }
  }
  if (tid < 80) {
    float y = 0.f;
#pragma unroll
    for (int w = 0; w < 8; ++w) y += Lf[L_Y + w * 80 + tid];
    __builtin_amdgcn_raw_buffer_store_b64(v2u{u_(y), tag}, rws, OFF_GA + (s * 32 + h) * 640 + tid * 8, 0, SC1);
  }
  if (wave == 2 && h < 20) {
    const int l1 = lane & 1;
    const int oc = 80 * s + 4 * h + 2 * l1;
    const u32 xp = *(const u32*)(p.x + oc);
    const int o2 = (s * 32 + (lane >> 1)) * 640 + (4 * h + 2 * l1) * 8;
    v4u a4, b4;
    bool ok = false;
#pragma unroll 1
    for (u32 it = 0; it < SPIN_G; ++it) {
      if (!ok) {
        a4 = __builtin_amdgcn_raw_buffer_load_b128(rws, o2, OFF_GA, SC1);
        b4 = __builtin_amdgcn_raw_buffer_load_b128(rws, o2, OFF_GB, SC1);
        ok = (a4.y == tag) & (a4.w == tag) & (b4.y == tag) & (b4.w == tag);
      }
      if (__all(ok)) break;
      NAP();
    }
    auto red = [&](float x) { x += dpp<X2>(x); x += dpp<0x124>(x); return rbf(sumhi(x)); };
    const float a0 = red(f_(a4.x)), a1 = red(f_(a4.z)), b0 = red(f_(b4.x)), b1 = red(f_(b4.z));
    const u32 r = bfbits(rbf(f_(xp << 16) + a0) + b0) | (bfbits(rbf(f_(xp & 0xffff0000u) + a1) + b1) << 16);
    if (lane < 2) *(u32*)(p.out + oc) = r;
    if (g == 0 && lane == 0) __builtin_amdgcn_raw_buffer_store_b32(tag, rws, OFF_E, 0, SC1);
    TS(8); TSW(8);
  }
}

extern "C" void kernel_launch(void* const* d_in, const int* in_sizes, int n_in, void* d_out, int out_size, void* d_ws, size_t ws_size,
                              hipStream_t stream) {
  Params p;
  p.x = (const u16*)d_in[0]; p.k = (const u16*)d_in[1]; p.v = (const u16*)d_in[2]; p.wq = (const u16*)d_in[3]; p.wo = (const u16*)d_in[4];
  p.wg = (const u16*)d_in[6]; p.wu = (const u16*)d_in[7]; p.wd = (const u16*)d_in[8];
  p.out = (u16*)d_out; p.ws = (unsigned char*)d_ws;
  hipLaunchKernelGGL(fused_decode, dim3(256), dim3(NTHR), 0, stream, p);
}
```

```cpp
#define DST 0
#include <hip/hip_runtime.h>
#include <hip/hip_bf16.h>

#define KV_AUX 2
#define W_AUX 2
#define WIN 8
#define HPRIO 1
#define ABL_WO 0
#define ABL_POLL 0
#define ABL_HELP 0
#define ABL_ST 0
#define STAMP 1

typedef unsigned u32;
typedef unsigned short u16;
typedef unsigned long long u64;
typedef u32 v2u __attribute__((ext_vector_type(2)));
typedef u32 v3u __attribute__((ext_vector_type(3)));
typedef u32 v4u __attribute__((ext_vector_type(4)));
typedef __bf16 bf16x2 __attribute__((ext_vector_type(2)));
#define LAS __attribute__((address_space(3)))
typedef __amdgpu_buffer_rsrc_t rsrc_t;

constexpr int D = 640, NH = 32, SK = 4096, DH = 192, FF = 1536, HD = NH * DH;
constexpr int NSTREAM = 8, NHELP = 4, NTHR = 64 * (NSTREAM + NHELP);
constexpr int SC1 = 16;
constexpr int OFF_E = 0;
constexpr int OFF_G0 = 4096, G0_STR = 2304;
constexpr int OFF_G1 = OFF_G0 + 256 * G0_STR, G1_STR = 1664;
constexpr int OFF_GA = OFF_G1 + 256 * G1_STR;
constexpr int OFF_GB = OFF_GA + 256 * 640;
constexpr int WS_END = OFF_GB + 256 * 640;
constexpr int OFF_STAMP = 2 << 20;
constexpr int L_CNT = 0, L_HQ = 16, L_HG = L_HQ + 4 * 192, L_QS = L_HG + 4 * 48, L_ACC = L_QS + 96, L_ML = L_ACC + 8 * 192,
              L_Y = L_ML + 16, L_END = L_Y + 8 * 80;
constexpr u32 SPIN_G = 1u << 17, SPIN_L = 1u << 21;

struct Params {
  const u16 *x, *k, *v, *wq, *wo, *wg, *wu, *wd;
  u16* out;
  unsigned char* ws;
};

__device__ __forceinline__ float f_(u32 u) { return __builtin_bit_cast(float, u); }
__device__ __forceinline__ u32 u_(float f) { return __builtin_bit_cast(u32, f); }
__device__ __forceinline__ float dot2(u32 a, u32 b, float c) {
  return __builtin_amdgcn_fdot2_f32_bf16(__builtin_bit_cast(bf16x2, a), __builtin_bit_cast(bf16x2, b), c, false);
}
__device__ __forceinline__ u32 bfbits(float f) { return (u32)__builtin_bit_cast(u16, (__bf16)f); }
__device__ __forceinline__ float rbf(float f) { return f_(bfbits(f) << 16); }
template <int CTRL> __device__ __forceinline__ float dpp(float x) {
  return f_((u32)__builtin_amdgcn_mov_dpp((int)u_(x), CTRL, 0xf, 0xf, true));
}
constexpr int X1 = 0xB1, X2 = 0x4E, X7 = 0x141, X8 = 0x128;
__device__ __forceinline__ float mrg16(float a, float b) {
  auto r = __builtin_amdgcn_permlane16_swap(u_(a), u_(b), false, false);
  return f_(r[0]) + f_(r[1]);
}
__device__ __forceinline__ float mrg32(float a, float b) {
  auto r = __builtin_amdgcn_permlane32_swap(u_(a), u_(b), false, false);
  return f_(r[0]) + f_(r[1]);
}
template <int CTRL> __device__ __forceinline__ float mrgd(float a, float b, bool bit) {
  const float ta = a + dpp<CTRL>(a), tb = b + dpp<CTRL>(b);
  return bit ? tb : ta;
}
__device__ __forceinline__ float sum8(float x) { x += dpp<X1>(x); x += dpp<X2>(x); x += dpp<X7>(x); return x; }
__device__ __forceinline__ float max8(float x) { x = fmaxf(x, dpp<X1>(x)); x = fmaxf(x, dpp<X2>(x)); x = fmaxf(x, dpp<X7>(x)); return x; }
__device__ __forceinline__ float sumhi(float x) { x += dpp<X8>(x); x = mrg16(x, x); return mrg32(x, x); }
__device__ __forceinline__ float maxhi(float x) {
  x = fmaxf(x, dpp<X8>(x));
  auto r = __builtin_amdgcn_permlane16_swap(u_(x), u_(x), false, false); x = fmaxf(f_(r[0]), f_(r[1]));
  auto t = __builtin_amdgcn_permlane32_swap(u_(x), u_(x), false, false); return fmaxf(f_(t[0]), f_(t[1]));
}
__device__ __forceinline__ float sum32(float x) { x = sum8(x); x += dpp<X8>(x); return mrg16(x, x); }
__device__ __forceinline__ float sum64(float x) { x = sum32(x); return mrg32(x, x); }
__device__ __forceinline__ float max64(float x) {
  x = max8(x); x = fmaxf(x, dpp<X8>(x));
  auto r = __builtin_amdgcn_permlane16_swap(u_(x), u_(x), false, false); x = fmaxf(f_(r[0]), f_(r[1]));
  auto t = __builtin_amdgcn_permlane32_swap(u_(x), u_(x), false, false); return fmaxf(f_(t[0]), f_(t[1]));
}
__device__ __forceinline__ rsrc_t mk(const void* p, int bytes) { return __builtin_amdgcn_make_buffer_rsrc((void*)p, 0, bytes, 0x00020000); }
#define LGKM0() asm volatile("s_waitcnt lgkmcnt(0)" ::: "memory")
#define BAR() do { asm volatile("s_waitcnt lgkmcnt(0)\n\ts_barrier" ::: "memory"); } while (0)
#define PACE(n) asm volatile("s_waitcnt vmcnt(%0)" ::"i"(n))
#define SB() __builtin_amdgcn_sched_barrier(0)
#define NAP() asm volatile("s_sleep 1" ::: "memory")
constexpr int brev5(int n) { return ((n & 1) << 4) | ((n & 2) << 2) | (n & 4) | ((n & 8) >> 2) | ((n & 16) >> 4); }

template <int N> struct IC { static constexpr int v = N; };
template <int I, int N, class Fn> __device__ __forceinline__ void sfor(Fn&& f) {
  if constexpr (I < N) { f(IC<I>{}); sfor<I + 1, N>(f); }
}
__device__ __forceinline__ void lds_wait_ge(volatile LAS u32* p, u32 want) {
#pragma unroll 1
  for (u32 i = 0; i < SPIN_L; ++i) { if (*p >= want) break; NAP(); }
  asm volatile("" ::: "memory");
}
__device__ __forceinline__ void lds_signal(LAS u32* p, int lane) {
  LGKM0();
  if (lane == 0) __hip_atomic_fetch_add(p, 1u, __ATOMIC_RELAXED, __HIP_MEMORY_SCOPE_WORKGROUP);
}
__device__ __forceinline__ void axpy10(float (&y)[10], v4u a, u32 b, u32 hb) {
  const u32 hh = hb << 16;
  y[0] = dot2(a.x, hb, y[0]); y[1] = dot2(a.x, hh, y[1]); y[2] = dot2(a.y, hb, y[2]); y[3] = dot2(a.y, hh, y[3]);
  y[4] = dot2(a.z, hb, y[4]); y[5] = dot2(a.z, hh, y[5]); y[6] = dot2(a.w, hb, y[6]); y[7] = dot2(a.w, hh, y[7]);
  y[8] = dot2(b, hb, y[8]); y[9] = dot2(b, hh, y[9]);
}
__device__ __forceinline__ void axpy6(float (&y)[6], v3u a, u32 hb) {
  const u32 hh = hb << 16;
  y[0] = dot2(a.x, hb, y[0]); y[1] = dot2(a.x, hh, y[1]); y[2] = dot2(a.y, hb, y[2]); y[3] = dot2(a.y, hh, y[3]);
  y[4] = dot2(a.z, hb, y[4]); y[5] = dot2(a.z, hh, y[5]);
}
__device__ __forceinline__ void red10(const float (&y)[10], int lane, float& v0, float& v1) {
  const float z0 = mrg32(y[0], y[1]), z1 = mrg32(y[2], y[3]), z2 = mrg32(y[4], y[5]), z3 = mrg32(y[6], y[7]), z4 = mrg32(y[8], y[9]);
  const float u0 = mrg16(z0, z1), u1 = mrg16(z2, z3), u2 = mrg16(z4, z4);
  v0 = mrgd<X8>(u0, u1, (lane & 8) != 0);
  v1 = u2 + dpp<X8>(u2);
}
#define TS(i) u64 ts##i; SB(); asm volatile("s_memrealtime %0\n\ts_waitcnt lgkmcnt(0)" : "=s"(ts##i) :: "memory"); SB()
#define TSW(i) do { if (i == DST) dly = ts##i + 4000; } while (0)
#define FIN() do { if (dly) { while (__builtin_amdgcn_s_memrealtime() < dly) __builtin_amdgcn_s_sleep(2); } } while (0)

__global__ void __launch_bounds__(NTHR, 3) fused_decode(Params p) {
  __shared__ __attribute__((aligned(16))) u32 lds_[L_END];
  LAS u32* L = (LAS u32*)lds_;
  LAS float* Lf = (LAS float*)lds_;
  const int tid = threadIdx.x, lane = tid & 63, wave = __builtin_amdgcn_readfirstlane(tid >> 6);
  const int b = blockIdx.x, h = (b & 7) * 4 + (b >> 6), s = (b >> 3) & 7, g = h * 8 + s;
  const int hf = lane >> 5, c = lane & 31, pg = lane >> 3, s8 = lane & 7;
  const rsrc_t rws = mk(p.ws, WS_END);
  TS(0);
  u64 dly = 0;
  asm volatile("" ::"s"(p.x), "s"(p.k), "s"(p.v), "s"(p.wq), "s"(p.wo), "s"(p.wg), "s"(p.wu), "s"(p.wd), "s"(p.out), "s"(p.ws));
  u32 eraw = __builtin_amdgcn_raw_buffer_load_b32(rws, OFF_E, 0, SC1);
  const int col0 = 8 * s8 + ((lane >> 1) & 4) + ((lane >> 3) & 2) + hf, col1 = 64 + 2 * s8 + hf;

  if (wave >= NSTREAM) {
    const int hw = wave - NSTREAM;
    __builtin_amdgcn_s_setprio(3);
    const rsrc_t rx = mk(p.x, D * 2);
    const v4u xa = __builtin_amdgcn_raw_buffer_load_b128(rx, lane * 16, 0, 0);
    const u32 xb = __builtin_amdgcn_raw_buffer_load_b32(rx, 1024 + lane * 4, 0, 0);
    const int myrow = 80 * s + (lane < 20 ? 20 * hw + lane : 40 * (hw & 1) + min(lane - 20, 39));
    const u32 xr = __builtin_amdgcn_raw_buffer_load_b16(rx, myrow * 2, 0, 0);
    const rsrc_t rq = mk(p.wq, (ABL_HELP & 2) ? 0 : D * HD * 2);
    const rsrc_t rg = mk(hw < 2 ? p.wg : p.wu, (ABL_HELP & 1) ? 0 : D * FF * 2);
    v3u wqv[10], guv[5];
    {
      const int o = (80 * s + 20 * hw + hf) * (HD * 2) + h * (DH * 2) + c * 12;
#pragma unroll
      for (int r = 0; r < 10; ++r) wqv[r] = __builtin_amdgcn_raw_buffer_load_b96(rq, o, r * 2 * HD * 2, W_AUX);
      const int o2 = (80 * s + 40 * (hw & 1) + pg) * (FF * 2) + h * 96 + s8 * 12;
#pragma unroll
      for (int r = 0; r < 5; ++r) guv[r] = __builtin_amdgcn_raw_buffer_load_b96(rg, o2, r * 8 * FF * 2, W_AUX);
    }
    if (hw == 0 && lane < 16) L[L_CNT + lane] = 0u;
    TS(10);
    BAR();
    SB();
    float xv[10];
    xv[0] = f_(xa.x << 16); xv[1] = f_(xa.x & 0xffff0000u); xv[2] = f_(xa.y << 16); xv[3] = f_(xa.y & 0xffff0000u);
    xv[4] = f_(xa.z << 16); xv[5] = f_(xa.z & 0xffff0000u); xv[6] = f_(xa.w << 16); xv[7] = f_(xa.w & 0xffff0000u);
    xv[8] = f_(xb << 16); xv[9] = f_(xb & 0xffff0000u);
    float sm = 0.f;
#pragma unroll
    for (int i = 0; i < 10; ++i) sm += xv[i];
    const float mean = sum64(sm) * (1.0f / D);
    float sq = 0.f;
#pragma unroll
    for (int i = 0; i < 10; ++i) { const float d = xv[i] - mean; sq = fmaf(d, d, sq); }
    const float var = sum64(sq) * (1.0f / D);
    const float den = rbf(sqrtf(rbf(rbf(var) + 1e-5f)));
    const u32 xn = bfbits(rbf(f_(xr << 16) - rbf(mean)) / den);
    float qa[6] = {0.f, 0.f, 0.f, 0.f, 0.f, 0.f}, ga[6] = {0.f, 0.f, 0.f, 0.f, 0.f, 0.f};
#pragma unroll
    for (int r = 0; r < 10; ++r) axpy6(qa, wqv[r], (u32)__builtin_amdgcn_ds_bpermute((2 * r + hf) * 4, (int)xn));
#pragma unroll
    for (int r = 0; r < 5; ++r) axpy6(ga, guv[r], (u32)__builtin_amdgcn_ds_bpermute((20 + 8 * r + pg) * 4, (int)xn));
#pragma unroll
    for (int i = 0; i < 3; ++i) {
      Lf[L_HQ + hw * 192 + 6 * c + 2 * i + hf] = mrg32(qa[2 * i], qa[2 * i + 1]);
      float t = mrg32(ga[2 * i], ga[2 * i + 1]);
      t = mrg16(t, t); t += dpp<X8>(t);
      if ((pg & 3) == 0) Lf[L_HG + hw * 48 + 6 * s8 + 2 * i + hf] = t;
    }
    asm volatile("" : "+v"(eraw));
    const u32 tag = eraw + 1u;
    lds_signal(L + L_CNT + 0, lane);
    lds_wait_ge(L + L_CNT + 0, 4u);
    if (hw < 3) {
      const int col = hw * 64 + lane;
      const float v = (Lf[L_HQ + col] + Lf[L_HQ + 192 + col]) + (Lf[L_HQ + 384 + col] + Lf[L_HQ + 576 + col]);
      __builtin_amdgcn_raw_buffer_store_b64(v2u{u_(v), tag}, rws, OFF_G0 + g * G0_STR + col * 8, 0, SC1);
      TS(1);
      const int o = OFF_G0 + (h * 8 + pg) * G0_STR + (64 * hw + 2 * s8) * 8;
      v4u q4[4];
      bool ok = false;
#pragma unroll 1
      for (u32 it = 0; it < SPIN_G; ++it) {
        if (!ok) {
#pragma unroll
          for (int i = 0; i < 4; ++i) q4[i] = __builtin_amdgcn_raw_buffer_load_b128(rws, o, i * 128, SC1);
          ok = true;
#pragma unroll
          for (int i = 0; i < 4; ++i) ok &= (q4[i].y == tag) & (q4[i].w == tag);
        }
        if (ABL_POLL || __all(ok)) break;
        NAP();
      }
#pragma unroll
      for (int i = 0; i < 4; ++i) {
        const u32 pk = bfbits(sumhi(f_(q4[i].x))) | (bfbits(sumhi(f_(q4[i].z))) << 16);
        if (pg == 0) L[L_QS + 32 * hw + 8 * i + s8] = pk;
      }
      lds_signal(L + L_CNT + 1, lane);
      TS(2);
      if (hw == 0) { TSW(1); TSW(2); }
    } else {
      if (lane < 48) {
        const float gv = Lf[L_HG + lane] + Lf[L_HG + 48 + lane], uv = Lf[L_HG + 96 + lane] + Lf[L_HG + 144 + lane];
        __builtin_amdgcn_raw_buffer_store_b128(v4u{u_(gv), tag, u_(uv), tag}, rws, OFF_G0 + g * G0_STR + 1536 + lane * 16, 0, SC1);
      }
      const rsrc_t rd = mk(p.wd, (ABL_HELP & 1) ? 0 : FF * D * 2);
      v4u da[6]; u32 db[6];
      const int od = (48 * h + pg) * (D * 2) + s * 160;
#pragma unroll
      for (int i = 0; i < 6; ++i) {
        da[i] = __builtin_amdgcn_raw_buffer_load_b128(rd, od + 16 * s8, i * 8 * D * 2, W_AUX);
        db[i] = __builtin_amdgcn_raw_buffer_load_b32(rd, od + 128 + 4 * s8, i * 8 * D * 2, W_AUX);
      }
      SB();
      const int o = OFF_G0 + (h * 8 + pg) * G0_STR + 1536 + s8 * 16;
      v4u g4[6];
      bool ok = false;
#pragma unroll 1
      for (u32 it = 0; it < SPIN_G; ++it) {
        if (!ok) {
#pragma unroll
          for (int i = 0; i < 6; ++i) g4[i] = __builtin_amdgcn_raw_buffer_load_b128(rws, o, i * 128, SC1);
          ok = true;
#pragma unroll
          for (int i = 0; i < 6; ++i) ok &= (g4[i].y == tag) & (g4[i].w == tag);
        }
        if (ABL_POLL || __all(ok)) break;
        NAP();
      }
      float y[10] = {0.f, 0.f, 0.f, 0.f, 0.f, 0.f, 0.f, 0.f, 0.f, 0.f};
#pragma unroll
      for (int i = 0; i < 6; ++i) {
        const float gg = rbf(sumhi(f_(g4[i].x))), uu = rbf(sumhi(f_(g4[i].z)));
        const float sg = rbf(1.0f / (1.0f + __expf(-gg)));
        axpy10(y, da[i], db[i], (u32)__builtin_amdgcn_ds_bpermute(pg * 4, (int)bfbits(sg * uu)));
      }
      float v0, v1;
      red10(y, lane, v0, v1);
      const int ob = OFF_GB + (s * 32 + h) * 640;
      __builtin_amdgcn_raw_buffer_store_b64(v2u{u_(v0), tag}, rws, ob + col0 * 8, 0, SC1);
      if ((lane & 24) == 0) __builtin_amdgcn_raw_buffer_store_b64(v2u{u_(v1), tag}, rws, ob + col1 * 8, 0, SC1);
      TS(9); TSW(9); TSW(10); { u64 ts11 = ts0; TSW(11); }
    }
    FIN();
    return;
  }

  const size_t kvo = ((size_t)h * SK + 512 * s) * (DH * 2);
  const rsrc_t rk = mk((const char*)p.k + kvo, 512 * DH * 2), rv = mk((const char*)p.v + kvo, 512 * DH * 2);
  const int vo = wave * (64 * DH * 2) + lane * 12;
  SB();
  BAR();
  TS(12);
  u64 ts13 = 0;
  v3u kv[32];
#pragma unroll
  for (int n = 0; n < 32; ++n) {
    if (n >= WIN) PACE(WIN - 1);
    if (n == WIN) { SB(); asm volatile("s_memrealtime %0\n\ts_waitcnt lgkmcnt(0)" : "=s"(ts13) :: "memory"); SB(); }
    kv[n] = __builtin_amdgcn_raw_buffer_load_b96(rk, vo, n * 768, KV_AUX);
    SB();
  }
  TS(3);
  lds_wait_ge(L + L_CNT + 1, 3u);
  TS(4);
  const u32 q0 = L[L_QS + 3 * c], q1 = L[L_QS + 3 * c + 1], q2 = L[L_QS + 3 * c + 2];
  float t1[16];
  constexpr int CPS = 32 / WIN;
  static_assert(CPS * WIN == 32 && CPS % 2 == 0, "window");
#pragma unroll
  for (int n = 0; n < WIN; ++n) {
#pragma unroll
    for (int a = n * CPS / 2; a < (n + 1) * CPS / 2; ++a) {
      const float s0 = dot2(kv[2 * a].z, q2, dot2(kv[2 * a].y, q1, dot2(kv[2 * a].x, q0, 0.f)));
      const float s1 = dot2(kv[2 * a + 1].z, q2, dot2(kv[2 * a + 1].y, q1, dot2(kv[2 * a + 1].x, q0, 0.f)));
      t1[a] = mrg16(s0, s1);
    }
    SB();
    PACE(WIN - 1);
    kv[n] = __builtin_amdgcn_raw_buffer_load_b96(rv, vo, n * 768, KV_AUX);
    SB();
  }
  kv[WIN] = __builtin_amdgcn_raw_buffer_load_b96(rv, vo, WIN * 768, KV_AUX);
  SB();
  float t2[8], t3[4];
#pragma unroll
  for (int a = 0; a < 8; ++a) t2[a] = mrgd<X8>(t1[2 * a], t1[2 * a + 1], (lane & 8) != 0);
#pragma unroll
  for (int a = 0; a < 4; ++a) t3[a] = mrgd<X7>(t2[2 * a], t2[2 * a + 1], (lane & 4) != 0);
  const float t40 = mrgd<X2>(t3[0], t3[1], (lane & 2) != 0), t41 = mrgd<X2>(t3[2], t3[3], (lane & 2) != 0);
  const float sc = rbf(rbf(mrgd<X1>(t40, t41, (lane & 1) != 0)) * 0.07216878364870322f);
  const float mw = max64(sc);
  const float e = __expf(sc - mw);
  const float lw = sum64(e);
  const u32 pb = bfbits(e);
  if (lane == 0) { Lf[L_ML + 2 * wave] = mw; Lf[L_ML + 2 * wave + 1] = lw; }
  const rsrc_t ro = mk(p.wo, ABL_WO ? 0 : HD * D * 2);
  const int oo = (DH * h + 24 * wave + pg) * (D * 2) + s * 160;
  v4u wa[3]; u32 wb[3];
  float acc[6] = {0.f, 0.f, 0.f, 0.f, 0.f, 0.f};
  sfor<0, 32>([&](auto ic) {
    constexpr int n = decltype(ic)::v;
    const u32 pn = (u32)__builtin_amdgcn_ds_swizzle((int)pb, brev5(n) << 5);
    axpy6(acc, kv[n], pn);
    SB();
    constexpr int m = n + WIN + 1;
    if constexpr (m < 32) kv[m] = __builtin_amdgcn_raw_buffer_load_b96(rv, vo, m * 768, KV_AUX);
    else if constexpr (m < 35) {
      wa[m - 32] = __builtin_amdgcn_raw_buffer_load_b128(ro, oo + 16 * s8, (m - 32) * 8 * D * 2, W_AUX);
      wb[m - 32] = __builtin_amdgcn_raw_buffer_load_b32(ro, oo + 128 + 4 * s8, (m - 32) * 8 * D * 2, W_AUX);
    }
    SB();
  });
#pragma unroll
  for (int i = 0; i < 3; ++i) Lf[L_ACC + wave * 192 + 6 * c + 2 * i + hf] = mrg32(acc[2 * i], acc[2 * i + 1]);
  asm volatile("" : "+v"(eraw));
  const u32 tag = eraw + 1u;
  TS(14);
  BAR();
  TS(5);
  if (wave < 4) {
    float mm[8], M = -INFINITY;
#pragma unroll
    for (int w = 0; w < 8; ++w) { mm[w] = Lf[L_ML + 2 * w]; M = fmaxf(M, mm[w]); }
    float o = 0.f;
    if (wave < 3) {
#pragma unroll
      for (int w = 0; w < 8; ++w) o = fmaf(__expf(mm[w] - M), Lf[L_ACC + w * 192 + tid], o);
      __builtin_amdgcn_raw_buffer_store_b64(v2u{u_(o), tag}, rws, OFF_G1 + g * G1_STR + tid * 8, 0, SC1);
    } else {
#pragma unroll
      for (int w = 0; w < 8; ++w) o = fmaf(__expf(mm[w] - M), Lf[L_ML + 2 * w + 1], o);
      if (lane == 0) __builtin_amdgcn_raw_buffer_store_b128(v4u{u_(M), tag, u_(o), tag}, rws, OFF_G1 + g * G1_STR + 1536, 0, SC1);
    }
  }
  {
    const int o1 = OFF_G1 + (h * 8 + pg) * G1_STR;
    v2u og[3]; v4u ml;
    bool ok = false;
#pragma unroll 1
    for (u32 it = 0; it < SPIN_G; ++it) {
      if (!ok) {
#pragma unroll
        for (int i = 0; i < 3; ++i) og[i] = __builtin_amdgcn_raw_buffer_load_b64(rws, o1 + (24 * wave + s8) * 8, i * 64, SC1);
        ml = __builtin_amdgcn_raw_buffer_load_b128(rws, o1 + 1536, 0, SC1);
        ok = (og[0].y == tag) & (og[1].y == tag) & (og[2].y == tag) & (ml.y == tag) & (ml.w == tag);
      }
      if (__all(ok)) break;
      NAP();
    }
    TS(6);
    const float M = maxhi(f_(ml.x));
    const float wgt = __expf(f_(ml.x) - M);
    const float inv = 1.0f / sumhi(wgt * f_(ml.z));
    float y[10] = {0.f, 0.f, 0.f, 0.f, 0.f, 0.f, 0.f, 0.f, 0.f, 0.f};
#pragma unroll
    for (int i = 0; i < 3; ++i)
      axpy10(y, wa[i], wb[i], (u32)__builtin_amdgcn_ds_bpermute(pg * 4, (int)bfbits(sumhi(wgt * f_(og[i].x)) * inv)));
    float v0, v1;
    red10(y, lane, v0, v1);
    Lf[L_Y + wave * 80 + col0] = v0;
    if ((lane & 24) == 0) Lf[L_Y + wave * 80 + col1] = v1;
    BAR();
    TS(7);
    if (wave == 4) { u64 ts15 = ts14; TSW(15); }
 if (wave == 0) { TSW(12); TSW(13); TSW(14); }
    if (wave == 0) { TSW(0); TSW(3); TSW(4); TSW(5); TSW(6); TSW(7); }
  }
  if (tid < 80) {
    float y = 0.f;
#pragma unroll
    for (int w = 0; w < 8; ++w) y += Lf[L_Y + w * 80 + tid];
    __builtin_amdgcn_raw_buffer_store_b64(v2u{u_(y), tag}, rws, OFF_GA + (s * 32 + h) * 640 + tid * 8, 0, SC1);
  }
  if (wave == 2 && h < 20) {
    const int l1 = lane & 1;
    const int oc = 80 * s + 4 * h + 2 * l1;
    const u32 xp = *(const u32*)(p.x + oc);
    const int o2 = (s * 32 + (lane >> 1)) * 640 + (4 * h + 2 * l1) * 8;
    v4u a4, b4;
    bool ok = false;
#pragma unroll 1
    for (u32 it = 0; it < SPIN_G; ++it) {
      if (!ok) {
        a4 = __builtin_amdgcn_raw_buffer_load_b128(rws, o2, OFF_GA, SC1);
        b4 = __builtin_amdgcn_raw_buffer_load_b128(rws, o2, OFF_GB, SC1);
        ok = (a4.y == tag) & (a4.w == tag) & (b4.y == tag) & (b4.w == tag);
      }
      if (__all(ok)) break;
      NAP();
    }
    auto red = [&](float x) { x += dpp<X2>(x); x += dpp<0x124>(x); return rbf(sumhi(x)); };
    const float a0 = red(f_(a4.x)), a1 = red(f_(a4.z)), b0 = red(f_(b4.x)), b1 = red(f_(b4.z));
    const u32 r = bfbits(rbf(f_(xp << 16) + a0) + b0) | (bfbits(rbf(f_(xp & 0xffff0000u) + a1) + b1) << 16);
    if (lane < 2) *(u32*)(p.out + oc) = r;
    if (g == 0 && lane == 0) __builtin_amdgcn_raw_buffer_store_b32(tag, rws, OFF_E, 0, SC1);
    TS(8); TSW(8);
  }
  FIN();
}

extern "C" void kernel_launch(void* const* d_in, const int* in_sizes, int n_in, void* d_out, int out_size, void* d_ws, size_t ws_size,
                              hipStream_t stream) {
  Params p;
  p.x = (const u16*)d_in[0]; p.k = (const u16*)d_in[1]; p.v = (const u16*)d_in[2]; p.wq = (const u16*)d_in[3]; p.wo = (const u16*)d_in[4];
  p.wg = (const u16*)d_in[6]; p.wu = (const u16*)d_in[7]; p.wd = (const u16*)d_in[8];
  p.out = (u16*)d_out; p.ws = (unsigned char*)d_ws;
  hipLaunchKernelGGL(fused_decode, dim3(256), dim3(NTHR), 0, stream, p);
}
```

```cpp
#define DST 16
#define DBLK 255
#include <hip/hip_runtime.h>
#include <hip/hip_bf16.h>

#define KV_AUX 2
#define W_AUX 2
#define WIN 8
#define HPRIO 1
#define ABL_WO 0
#define ABL_POLL 0
#define ABL_HELP 0
#define ABL_ST 0
#define STAMP 1

typedef unsigned u32;
typedef unsigned short u16;
typedef unsigned long long u64;
typedef u32 v2u __attribute__((ext_vector_type(2)));
typedef u32 v3u __attribute__((ext_vector_type(3)));
typedef u32 v4u __attribute__((ext_vector_type(4)));
typedef __bf16 bf16x2 __attribute__((ext_vector_type(2)));
#define LAS __attribute__((address_space(3)))
typedef __amdgpu_buffer_rsrc_t rsrc_t;

constexpr int D = 640, NH = 32, SK = 4096, DH = 192, FF = 1536, HD = NH * DH;
constexpr int NSTREAM = 8, NHELP = 4, NTHR = 64 * (NSTREAM + NHELP);
constexpr int SC1 = 16;
constexpr int OFF_E = 0;
constexpr int OFF_G0 = 4096, G0_STR = 2304;
constexpr int OFF_G1 = OFF_G0 + 256 * G0_STR, G1_STR = 1664;
constexpr int OFF_GA = OFF_G1 + 256 * G1_STR;
constexpr int OFF_GB = OFF_GA + 256 * 640;
constexpr int WS_END = OFF_GB + 256 * 640;
constexpr int OFF_STAMP = 2 << 20;
constexpr int L_CNT = 0, L_HQ = 16, L_HG = L_HQ + 4 * 192, L_QS = L_HG + 4 * 48, L_ACC = L_QS + 96, L_ML = L_ACC + 8 * 192,
              L_Y = L_ML + 16, L_END = L_Y + 8 * 80;
constexpr u32 SPIN_G = 1u << 17, SPIN_L = 1u << 21;

struct Params {
  const u16 *x, *k, *v, *wq, *wo, *wg, *wu, *wd;
  u16* out;
  unsigned char* ws;
};

__device__ __forceinline__ float f_(u32 u) { return __builtin_bit_cast(float, u); }
__device__ __forceinline__ u32 u_(float f) { return __builtin_bit_cast(u32, f); }
__device__ __forceinline__ float dot2(u32 a, u32 b, float c) {
  return __builtin_amdgcn_fdot2_f32_bf16(__builtin_bit_cast(bf16x2, a), __builtin_bit_cast(bf16x2, b), c, false);
}
__device__ __forceinline__ u32 bfbits(float f) { return (u32)__builtin_bit_cast(u16, (__bf16)f); }
__device__ __forceinline__ float rbf(float f) { return f_(bfbits(f) << 16); }
template <int CTRL> __device__ __forceinline__ float dpp(float x) {
  return f_((u32)__builtin_amdgcn_mov_dpp((int)u_(x), CTRL, 0xf, 0xf, true));
}
constexpr int X1 = 0xB1, X2 = 0x4E, X7 = 0x141, X8 = 0x128;
__device__ __forceinline__ float mrg16(float a, float b) {
  auto r = __builtin_amdgcn_permlane16_swap(u_(a), u_(b), false, false);
  return f_(r[0]) + f_(r[1]);
}
__device__ __forceinline__ float mrg32(float a, float b) {
  auto r = __builtin_amdgcn_permlane32_swap(u_(a), u_(b), false, false);
  return f_(r[0]) + f_(r[1]);
}
template <int CTRL> __device__ __forceinline__ float mrgd(float a, float b, bool bit) {
  const float ta = a + dpp<CTRL>(a), tb = b + dpp<CTRL>(b);
  return bit ? tb : ta;
}
__device__ __forceinline__ float sum8(float x) { x += dpp<X1>(x); x += dpp<X2>(x); x += dpp<X7>(x); return x; }
__device__ __forceinline__ float max8(float x) { x = fmaxf(x, dpp<X1>(x)); x = fmaxf(x, dpp<X2>(x)); x = fmaxf(x, dpp<X7>(x)); return x; }
__device__ __forceinline__ float sumhi(float x) { x += dpp<X8>(x); x = mrg16(x, x); return mrg32(x, x); }
__device__ __forceinline__ float maxhi(float x) {
  x = fmaxf(x, dpp<X8>(x));
  auto r = __builtin_amdgcn_permlane16_swap(u_(x), u_(x), false, false); x = fmaxf(f_(r[0]), f_(r[1]));
  auto t = __builtin_amdgcn_permlane32_swap(u_(x), u_(x), false, false); return fmaxf(f_(t[0]), f_(t[1]));
}
__device__ __forceinline__ float sum32(float x) { x = sum8(x); x += dpp<X8>(x); return mrg16(x, x); }
__device__ __forceinline__ float sum64(float x) { x = sum32(x); return mrg32(x, x); }
__device__ __forceinline__ float max64(float x) {
  x = max8(x); x = fmaxf(x, dpp<X8>(x));
  auto r = __builtin_amdgcn_permlane16_swap(u_(x), u_(x), false, false); x = fmaxf(f_(r[0]), f_(r[1]));
  auto t = __builtin_amdgcn_permlane32_swap(u_(x), u_(x), false, false); return fmaxf(f_(t[0]), f_(t[1]));
}
__device__ __forceinline__ rsrc_t mk(const void* p, int bytes) { return __builtin_amdgcn_make_buffer_rsrc((void*)p, 0, bytes, 0x00020000); }
#define LGKM0() asm volatile("s_waitcnt lgkmcnt(0)" ::: "memory")
#define BAR() do { asm volatile("s_waitcnt lgkmcnt(0)\n\ts_barrier" ::: "memory"); } while (0)
#define PACE(n) asm volatile("s_waitcnt vmcnt(%0)" ::"i"(n))
#define SB() __builtin_amdgcn_sched_barrier(0)
#define NAP() asm volatile("s_sleep 1" ::: "memory")
constexpr int brev5(int n) { return ((n & 1) << 4) | ((n & 2) << 2) | (n & 4) | ((n & 8) >> 2) | ((n & 16) >> 4); }

template <int N> struct IC { static constexpr int v = N; };
template <int I, int N, class Fn> __device__ __forceinline__ void sfor(Fn&& f) {
  if constexpr (I < N) { f(IC<I>{}); sfor<I + 1, N>(f); }
}
__device__ __forceinline__ void lds_wait_ge(volatile LAS u32* p, u32 want) {
#pragma unroll 1
  for (u32 i = 0; i < SPIN_L; ++i) { if (*p >= want) break; NAP(); }
  asm volatile("" ::: "memory");
}
__device__ __forceinline__ void lds_signal(LAS u32* p, int lane) {
  LGKM0();
  if (lane == 0) __hip_atomic_fetch_add(p, 1u, __ATOMIC_RELAXED, __HIP_MEMORY_SCOPE_WORKGROUP);
}
__device__ __forceinline__ void axpy10(float (&y)[10], v4u a, u32 b, u32 hb) {
  const u32 hh = hb << 16;
  y[0] = dot2(a.x, hb, y[0]); y[1] = dot2(a.x, hh, y[1]); y[2] = dot2(a.y, hb, y[2]); y[3] = dot2(a.y, hh, y[3]);
  y[4] = dot2(a.z, hb, y[4]); y[5] = dot2(a.z, hh, y[5]); y[6] = dot2(a.w, hb, y[6]); y[7] = dot2(a.w, hh, y[7]);
  y[8] = dot2(b, hb, y[8]); y[9] = dot2(b, hh, y[9]);
}
__device__ __forceinline__ void axpy6(float (&y)[6], v3u a, u32 hb) {
  const u32 hh = hb << 16;
  y[0] = dot2(a.x, hb, y[0]); y[1] = dot2(a.x, hh, y[1]); y[2] = dot2(a.y, hb, y[2]); y[3] = dot2(a.y, hh, y[3]);
  y[4] = dot2(a.z, hb, y[4]); y[5] = dot2(a.z, hh, y[5]);
}
__device__ __forceinline__ void red10(const float (&y)[10], int lane, float& v0, float& v1) {
  const float z0 = mrg32(y[0], y[1]), z1 = mrg32(y[2], y[3]), z2 = mrg32(y[4], y[5]), z3 = mrg32(y[6], y[7]), z4 = mrg32(y[8], y[9]);
  const float u0 = mrg16(z0, z1), u1 = mrg16(z2, z3), u2 = mrg16(z4, z4);
  v0 = mrgd<X8>(u0, u1, (lane & 8) != 0);
  v1 = u2 + dpp<X8>(u2);
}
#define TS(i) u64 ts##i; SB(); asm volatile("s_memrealtime %0\n\ts_waitcnt lgkmcnt(0)" : "=s"(ts##i) :: "memory"); SB()
#define TSW(i) do { if (i == DST && (DBLK < 0 || (int)blockIdx.x == DBLK)) dly = ts##i + 4000; } while (0)
#define FIN() do { if (dly) { while (__builtin_amdgcn_s_memrealtime() < dly) __builtin_amdgcn_s_sleep(2); } } while (0)

__global__ void __launch_bounds__(NTHR, 3) fused_decode(Params p_) {
  typedef __attribute__((address_space(4))) const Params* KP;
  KP kp = (KP)__builtin_amdgcn_kernarg_segment_ptr();
  u64 ts16;
  asm volatile("s_memrealtime %0\n\ts_waitcnt lgkmcnt(0)" : "=s"(ts16), "+s"(kp) :: "memory");
  Params p;
  p.x = kp->x; p.k = kp->k; p.v = kp->v; p.wq = kp->wq; p.wo = kp->wo; p.wg = kp->wg; p.wu = kp->wu; p.wd = kp->wd; p.out = kp->out; p.ws = kp->ws;
  __shared__ __attribute__((aligned(16))) u32 lds_[L_END];
  LAS u32* L = (LAS u32*)lds_;
  LAS float* Lf = (LAS float*)lds_;
  const int tid = threadIdx.x, lane = tid & 63, wave = __builtin_amdgcn_readfirstlane(tid >> 6);
  const int b = blockIdx.x, h = (b & 7) * 4 + (b >> 6), s = (b >> 3) & 7, g = h * 8 + s;
  const int hf = lane >> 5, c = lane & 31, pg = lane >> 3, s8 = lane & 7;
  const rsrc_t rws = mk(p.ws, WS_END);
  TS(0);
  u64 dly = 0;
  asm volatile("" ::"s"(p.x), "s"(p.k), "s"(p.v), "s"(p.wq), "s"(p.wo), "s"(p.wg), "s"(p.wu), "s"(p.wd), "s"(p.out), "s"(p.ws));
  u32 eraw = __builtin_amdgcn_raw_buffer_load_b32(rws, OFF_E, 0, SC1);
  const int col0 = 8 * s8 + ((lane >> 1) & 4) + ((lane >> 3) & 2) + hf, col1 = 64 + 2 * s8 + hf;

  if (wave >= NSTREAM) {
    const int hw = wave - NSTREAM;
    __builtin_amdgcn_s_setprio(3);
    const rsrc_t rx = mk(p.x, D * 2);
    const v4u xa = __builtin_amdgcn_raw_buffer_load_b128(rx, lane * 16, 0, 0);
    const u32 xb = __builtin_amdgcn_raw_buffer_load_b32(rx, 1024 + lane * 4, 0, 0);
    const int myrow = 80 * s + (lane < 20 ? 20 * hw + lane : 40 * (hw & 1) + min(lane - 20, 39));
    const u32 xr = __builtin_amdgcn_raw_buffer_load_b16(rx, myrow * 2, 0, 0);
    const rsrc_t rq = mk(p.wq, (ABL_HELP & 2) ? 0 : D * HD * 2);
    const rsrc_t rg = mk(hw < 2 ? p.wg : p.wu, (ABL_HELP & 1) ? 0 : D * FF * 2);
    v3u wqv[10], guv[5];
    {
      const int o = (80 * s + 20 * hw + hf) * (HD * 2) + h * (DH * 2) + c * 12;
#pragma unroll
      for (int r = 0; r < 10; ++r) wqv[r] = __builtin_amdgcn_raw_buffer_load_b96(rq, o, r * 2 * HD * 2, W_AUX);
      const int o2 = (80 * s + 40 * (hw & 1) + pg) * (FF * 2) + h * 96 + s8 * 12;
#pragma unroll
      for (int r = 0; r < 5; ++r) guv[r] = __builtin_amdgcn_raw_buffer_load_b96(rg, o2, r * 8 * FF * 2, W_AUX);
    }
    if (hw == 0 && lane < 16) L[L_CNT + lane] = 0u;
    TS(10);
    BAR();
    SB();
    float xv[10];
    xv[0] = f_(xa.x << 16); xv[1] = f_(xa.x & 0xffff0000u); xv[2] = f_(xa.y << 16); xv[3] = f_(xa.y & 0xffff0000u);
    xv[4] = f_(xa.z << 16); xv[5] = f_(xa.z & 0xffff0000u); xv[6] = f_(xa.w << 16); xv[7] = f_(xa.w & 0xffff0000u);
    xv[8] = f_(xb << 16); xv[9] = f_(xb & 0xffff0000u);
    float sm = 0.f;
#pragma unroll
    for (int i = 0; i < 10; ++i) sm += xv[i];
    const float mean = sum64(sm) * (1.0f / D);
    float sq = 0.f;
#pragma unroll
    for (int i = 0; i < 10; ++i) { const float d = xv[i] - mean; sq = fmaf(d, d, sq); }
    const float var = sum64(sq) * (1.0f / D);
    const float den = rbf(sqrtf(rbf(rbf(var) + 1e-5f)));
    const u32 xn = bfbits(rbf(f_(xr << 16) - rbf(mean)) / den);
    float qa[6] = {0.f, 0.f, 0.f, 0.f, 0.f, 0.f}, ga[6] = {0.f, 0.f, 0.f, 0.f, 0.f, 0.f};
#pragma unroll
    for (int r = 0; r < 10; ++r) axpy6(qa, wqv[r], (u32)__builtin_amdgcn_ds_bpermute((2 * r + hf) * 4, (int)xn));
#pragma unroll
    for (int r = 0; r < 5; ++r) axpy6(ga, guv[r], (u32)__builtin_amdgcn_ds_bpermute((20 + 8 * r + pg) * 4, (int)xn));
#pragma unroll
    for (int i = 0; i < 3; ++i) {
      Lf[L_HQ + hw * 192 + 6 * c + 2 * i + hf] = mrg32(qa[2 * i], qa[2 * i + 1]);
      float t = mrg32(ga[2 * i], ga[2 * i + 1]);
      t = mrg16(t, t); t += dpp<X8>(t);
      if ((pg & 3) == 0) Lf[L_HG + hw * 48 + 6 * s8 + 2 * i + hf] = t;
    }
    asm volatile("" : "+v"(eraw));
    const u32 tag = eraw + 1u;
    lds_signal(L + L_CNT + 0, lane);
    lds_wait_ge(L + L_CNT + 0, 4u);
    if (hw < 3) {
      const int col = hw * 64 + lane;
      const float v = (Lf[L_HQ + col] + Lf[L_HQ + 192 + col]) + (Lf[L_HQ + 384 + col] + Lf[L_HQ + 576 + col]);
      __builtin_amdgcn_raw_buffer_store_b64(v2u{u_(v), tag}, rws, OFF_G0 + g * G0_STR + col * 8, 0, SC1);
      TS(1);
      const int o = OFF_G0 + (h * 8 + pg) * G0_STR + (64 * hw + 2 * s8) * 8;
      v4u q4[4];
      bool ok = false;
#pragma unroll 1
      for (u32 it = 0; it < SPIN_G; ++it) {
        if (!ok) {
#pragma unroll
          for (int i = 0; i < 4; ++i) q4[i] = __builtin_amdgcn_raw_buffer_load_b128(rws, o, i * 128, SC1);
          ok = true;
#pragma unroll
          for (int i = 0; i < 4; ++i) ok &= (q4[i].y == tag) & (q4[i].w == tag);
        }
        if (ABL_POLL || __all(ok)) break;
        NAP();
      }
#pragma unroll
      for (int i = 0; i < 4; ++i) {
        const u32 pk = bfbits(sumhi(f_(q4[i].x))) | (bfbits(sumhi(f_(q4[i].z))) << 16);
        if (pg == 0) L[L_QS + 32 * hw + 8 * i + s8] = pk;
      }
      lds_signal(L + L_CNT + 1, lane);
      TS(2);
      if (hw == 0) { TSW(1); TSW(2); }
    } else {
      if (lane < 48) {
        const float gv = Lf[L_HG + lane] + Lf[L_HG + 48 + lane], uv = Lf[L_HG + 96 + lane] + Lf[L_HG + 144 + lane];
        __builtin_amdgcn_raw_buffer_store_b128(v4u{u_(gv), tag, u_(uv), tag}, rws, OFF_G0 + g * G0_STR + 1536 + lane * 16, 0, SC1);
      }
      const rsrc_t rd = mk(p.wd, (ABL_HELP & 1) ? 0 : FF * D * 2);
      v4u da[6]; u32 db[6];
      const int od = (48 * h + pg) * (D * 2) + s * 160;
#pragma unroll
      for (int i = 0; i < 6; ++i) {
        da[i] = __builtin_amdgcn_raw_buffer_load_b128(rd, od + 16 * s8, i * 8 * D * 2, W_AUX);
        db[i] = __builtin_amdgcn_raw_buffer_load_b32(rd, od + 128 + 4 * s8, i * 8 * D * 2, W_AUX);
      }
      SB();
      const int o = OFF_G0 + (h * 8 + pg) * G0_STR + 1536 + s8 * 16;
      v4u g4[6];
      bool ok = false;
#pragma unroll 1
      for (u32 it = 0; it < SPIN_G; ++it) {
        if (!ok) {
#pragma unroll
          for (int i = 0; i < 6; ++i) g4[i] = __builtin_amdgcn_raw_buffer_load_b128(rws, o, i * 128, SC1);
          ok = true;
#pragma unroll
          for (int i = 0; i < 6; ++i) ok &= (g4[i].y == tag) & (g4[i].w == tag);
        }
        if (ABL_POLL || __all(ok)) break;
        NAP();
      }
      float y[10] = {0.f, 0.f, 0.f, 0.f, 0.f, 0.f, 0.f, 0.f, 0.f, 0.f};
#pragma unroll
      for (int i = 0; i < 6; ++i) {
        const float gg = rbf(sumhi(f_(g4[i].x))), uu = rbf(sumhi(f_(g4[i].z)));
        const float sg = rbf(1.0f / (1.0f + __expf(-gg)));
        axpy10(y, da[i], db[i], (u32)__builtin_amdgcn_ds_bpermute(pg * 4, (int)bfbits(sg * uu)));
      }
      float v0, v1;
      red10(y, lane, v0, v1);
      const int ob = OFF_GB + (s * 32 + h) * 640;
      __builtin_amdgcn_raw_buffer_store_b64(v2u{u_(v0), tag}, rws, ob + col0 * 8, 0, SC1);
      if ((lane & 24) == 0) __builtin_amdgcn_raw_buffer_store_b64(v2u{u_(v1), tag}, rws, ob + col1 * 8, 0, SC1);
      TS(9); TSW(9); TSW(10); { u64 ts11 = ts0; TSW(11); }
    }
    FIN();
    return;
  }

  const size_t kvo = ((size_t)h * SK + 512 * s) * (DH * 2);
  const rsrc_t rk = mk((const char*)p.k + kvo, 512 * DH * 2), rv = mk((const char*)p.v + kvo, 512 * DH * 2);
  const int vo = wave * (64 * DH * 2) + lane * 12;
  SB();
  BAR();
  TS(12);
  u64 ts13 = 0;
  v3u kv[32];
#pragma unroll
  for (int n = 0; n < 32; ++n) {
    if (n >= WIN) PACE(WIN - 1);
    if (n == WIN) { SB(); asm volatile("s_memrealtime %0\n\ts_waitcnt lgkmcnt(0)" : "=s"(ts13) :: "memory"); SB(); }
    kv[n] = __builtin_amdgcn_raw_buffer_load_b96(rk, vo, n * 768, KV_AUX);
    SB();
  }
  TS(3);
  lds_wait_ge(L + L_CNT + 1, 3u);
  TS(4);
  const u32 q0 = L[L_QS + 3 * c], q1 = L[L_QS + 3 * c + 1], q2 = L[L_QS + 3 * c + 2];
  float t1[16];
  constexpr int CPS = 32 / WIN;
  static_assert(CPS * WIN == 32 && CPS % 2 == 0, "window");
#pragma unroll
  for (int n = 0; n < WIN; ++n) {
#pragma unroll
    for (int a = n * CPS / 2; a < (n + 1) * CPS / 2; ++a) {
      const float s0 = dot2(kv[2 * a].z, q2, dot2(kv[2 * a].y, q1, dot2(kv[2 * a].x, q0, 0.f)));
      const float s1 = dot2(kv[2 * a + 1].z, q2, dot2(kv[2 * a + 1].y, q1, dot2(kv[2 * a + 1].x, q0, 0.f)));
      t1[a] = mrg16(s0, s1);
    }
    SB();
    PACE(WIN - 1);
    kv[n] = __builtin_amdgcn_raw_buffer_load_b96(rv, vo, n * 768, KV_AUX);
    SB();
  }
  kv[WIN] = __builtin_amdgcn_raw_buffer_load_b96(rv, vo, WIN * 768, KV_AUX);
  SB();
  float t2[8], t3[4];
#pragma unroll
  for (int a = 0; a < 8; ++a) t2[a] = mrgd<X8>(t1[2 * a], t1[2 * a + 1], (lane & 8) != 0);
#pragma unroll
  for (int a = 0; a < 4; ++a) t3[a] = mrgd<X7>(t2[2 * a], t2[2 * a + 1], (lane & 4) != 0);
  const float t40 = mrgd<X2>(t3[0], t3[1], (lane & 2) != 0), t41 = mrgd<X2>(t3[2], t3[3], (lane & 2) != 0);
  const float sc = rbf(rbf(mrgd<X1>(t40, t41, (lane & 1) != 0)) * 0.07216878364870322f);
  const float mw = max64(sc);
  const float e = __expf(sc - mw);
  const float lw = sum64(e);
  const u32 pb = bfbits(e);
  if (lane == 0) { Lf[L_ML + 2 * wave] = mw; Lf[L_ML + 2 * wave + 1] = lw; }
  const rsrc_t ro = mk(p.wo, ABL_WO ? 0 : HD * D * 2);
  const int oo = (DH * h + 24 * wave + pg) * (D * 2) + s * 160;
  v4u wa[3]; u32 wb[3];
  float acc[6] = {0.f, 0.f, 0.f, 0.f, 0.f, 0.f};
  sfor<0, 32>([&](auto ic) {
    constexpr int n = decltype(ic)::v;
    const u32 pn = (u32)__builtin_amdgcn_ds_swizzle((int)pb, brev5(n) << 5);
    axpy6(acc, kv[n], pn);
    SB();
    constexpr int m = n + WIN + 1;
    if constexpr (m < 32) kv[m] = __builtin_amdgcn_raw_buffer_load_b96(rv, vo, m * 768, KV_AUX);
    else if constexpr (m < 35) {
      wa[m - 32] = __builtin_amdgcn_raw_buffer_load_b128(ro, oo + 16 * s8, (m - 32) * 8 * D * 2, W_AUX);
      wb[m - 32] = __builtin_amdgcn_raw_buffer_load_b32(ro, oo + 128 + 4 * s8, (m - 32) * 8 * D * 2, W_AUX);
    }
    SB();
  });
#pragma unroll
  for (int i = 0; i < 3; ++i) Lf[L_ACC + wave * 192 + 6 * c + 2 * i + hf] = mrg32(acc[2 * i], acc[2 * i + 1]);
  asm volatile("" : "+v"(eraw));
  const u32 tag = eraw + 1u;
  TS(14);
  BAR();
  TS(5);
  if (wave < 4) {
    float mm[8], M = -INFINITY;
#pragma unroll
    for (int w = 0; w < 8; ++w) { mm[w] = Lf[L_ML + 2 * w]; M = fmaxf(M, mm[w]); }
    float o = 0.f;
    if (wave < 3) {
#pragma unroll
      for (int w = 0; w < 8; ++w) o = fmaf(__expf(mm[w] - M), Lf[L_ACC + w * 192 + tid], o);
      __builtin_amdgcn_raw_buffer_store_b64(v2u{u_(o), tag}, rws, OFF_G1 + g * G1_STR + tid * 8, 0, SC1);
    } else {
#pragma unroll
      for (int w = 0; w < 8; ++w) o = fmaf(__expf(mm[w] - M), Lf[L_ML + 2 * w + 1], o);
      if (lane == 0) __builtin_amdgcn_raw_buffer_store_b128(v4u{u_(M), tag, u_(o), tag}, rws, OFF_G1 + g * G1_STR + 1536, 0, SC1);
    }
  }
  {
    const int o1 = OFF_G1 + (h * 8 + pg) * G1_STR;
    v2u og[3]; v4u ml;
    bool ok = false;
#pragma unroll 1
    for (u32 it = 0; it < SPIN_G; ++it) {
      if (!ok) {
#pragma unroll
        for (int i = 0; i < 3; ++i) og[i] = __builtin_amdgcn_raw_buffer_load_b64(rws, o1 + (24 * wave + s8) * 8, i * 64, SC1);
        ml = __builtin_amdgcn_raw_buffer_load_b128(rws, o1 + 1536, 0, SC1);
        ok = (og[0].y == tag) & (og[1].y == tag) & (og[2].y == tag) & (ml.y == tag) & (ml.w == tag);
      }
      if (__all(ok)) break;
      NAP();
    }
    TS(6);
    const float M = maxhi(f_(ml.x));
    const float wgt = __expf(f_(ml.x) - M);
    const float inv = 1.0f / sumhi(wgt * f_(ml.z));
    float y[10] = {0.f, 0.f, 0.f, 0.f, 0.f, 0.f, 0.f, 0.f, 0.f, 0.f};
#pragma unroll
    for (int i = 0; i < 3; ++i)
      axpy10(y, wa[i], wb[i], (u32)__builtin_amdgcn_ds_bpermute(pg * 4, (int)bfbits(sumhi(wgt * f_(og[i].x)) * inv)));
    float v0, v1;
    red10(y, lane, v0, v1);
    Lf[L_Y + wave * 80 + col0] = v0;
    if ((lane & 24) == 0) Lf[L_Y + wave * 80 + col1] = v1;
    BAR();
    TS(7);
    if (wave == 4) { u64 ts15 = ts14; TSW(15); }
 if (wave == 0) { TSW(12); TSW(13); TSW(14); }
    if (wave == 0) { TSW(16); TSW(0); TSW(3); TSW(4); TSW(5); TSW(6); TSW(7); }
  }
  if (tid < 80) {
    float y = 0.f;
#pragma unroll
    for (int w = 0; w < 8; ++w) y += Lf[L_Y + w * 80 + tid];
    __builtin_amdgcn_raw_buffer_store_b64(v2u{u_(y), tag}, rws, OFF_GA + (s * 32 + h) * 640 + tid * 8, 0, SC1);
  }
  if (wave == 2 && h < 20) {
    const int l1 = lane & 1;
    const int oc = 80 * s + 4 * h + 2 * l1;
    const u32 xp = *(const u32*)(p.x + oc);
    const int o2 = (s * 32 + (lane >> 1)) * 640 + (4 * h + 2 * l1) * 8;
    v4u a4, b4;
    bool ok = false;
#pragma unroll 1
    for (u32 it = 0; it < SPIN_G; ++it) {
      if (!ok) {
        a4 = __builtin_amdgcn_raw_buffer_load_b128(rws, o2, OFF_GA, SC1);
        b4 = __builtin_amdgcn_raw_buffer_load_b128(rws, o2, OFF_GB, SC1);
        ok = (a4.y == tag) & (a4.w == tag) & (b4.y == tag) & (b4.w == tag);
      }
      if (__all(ok)) break;
      NAP();
    }
    auto red = [&](float x) { x += dpp<X2>(x); x += dpp<0x124>(x); return rbf(sumhi(x)); };
    const float a0 = red(f_(a4.x)), a1 = red(f_(a4.z)), b0 = red(f_(b4.x)), b1 = red(f_(b4.z));
    const u32 r = bfbits(rbf(f_(xp << 16) + a0) + b0) | (bfbits(rbf(f_(xp & 0xffff0000u) + a1) + b1) << 16);
    if (lane < 2) *(u32*)(p.out + oc) = r;
    if (g == 0 && lane == 0) __builtin_amdgcn_raw_buffer_store_b32(tag, rws, OFF_E, 0, SC1);
    TS(8); TSW(8);
  }
  FIN();
}

extern "C" void kernel_launch(void* const* d_in, const int* in_sizes, int n_in, void* d_out, int out_size, void* d_ws, size_t ws_size,
                              hipStream_t stream) {
  Params p;
  p.x = (const u16*)d_in[0]; p.k = (const u16*)d_in[1]; p.v = (const u16*)d_in[2]; p.wq = (const u16*)d_in[3]; p.wo = (const u16*)d_in[4];
  p.wg = (const u16*)d_in[6]; p.wu = (const u16*)d_in[7]; p.wd = (const u16*)d_in[8];
  p.out = (u16*)d_out; p.ws = (unsigned char*)d_ws;
  hipLaunchKernelGGL(fused_decode, dim3(256), dim3(NTHR), 0, stream, p);
}
```

```cpp
#include <hip/hip_runtime.h>
#include <hip/hip_bf16.h>

#define STAMP 0
#define FORCE_FB 0
#define CODE_BYTES 20480

typedef unsigned u32;
typedef unsigned short u16;
typedef unsigned long long u64;
typedef u32 v2u __attribute__((ext_vector_type(2)));
typedef u32 v3u __attribute__((ext_vector_type(3)));
typedef u32 v4u __attribute__((ext_vector_type(4)));
typedef float v4f __attribute__((ext_vector_type(4)));
typedef u64 v8q __attribute__((ext_vector_type(8)));
typedef u64 v2q __attribute__((ext_vector_type(2)));
typedef __bf16 bf16x2 __attribute__((ext_vector_type(2)));
typedef __bf16 bf16x8 __attribute__((ext_vector_type(8)));
#define LAS __attribute__((address_space(3)))
#define GAS __attribute__((address_space(1)))
typedef GAS u64 gu64;
typedef __amdgpu_buffer_rsrc_t rsrc_t;

constexpr int D = 640, NH = 32, SK = 4096, DH = 192, FF = 1536, HD = NH * DH;
constexpr int NSTREAM = 8, NHELP = 4, NTHR = 64 * (NSTREAM + NHELP);
constexpr int SC1 = 16, NT = 2, OOB = 0x7ff00000;
constexpr int HEAD = 2;
constexpr int TOFF = 190, TPACE = 1380;
constexpr int PH2 = 24;
constexpr int WIN = 7, TTAIL = 14;
constexpr int RN = 5, NREP = 32;
constexpr int CA1 = 400, CR1 = 150, CT2 = 720, CTD = 200, CRD = 60;
constexpr int OFF_E = 0;
constexpr int OFF_GF = 1024;
constexpr int OFF_GT = 2048;
constexpr int OFF_G0 = 8192, G0_STR = 1536;
constexpr int OFF_G1 = OFF_G0 + 256 * G0_STR, G1_STR = 1664;
constexpr int OFF_GA = OFF_G1 + 256 * G1_STR;
constexpr int OFF_AO = OFF_GA + 256 * 640;
constexpr int OFF_AY = OFF_AO + 32 * 24 * 256;
constexpr int WS_END = OFF_AY + 80 * 256;
constexpr int OFF_STAMP = 2 << 20;
constexpr int L_CNT = 0, L_DUE = 16, L_SH = 24;
constexpr int L_HP = 32, L_QS = L_HP + 4 * 3 * 128, L_E = L_QS + 96, L_ACC = L_E + 4, L_ML = L_ACC + 8 * 192 + 64, L_FW = L_ML + 16,
              L_Y = L_FW + 8, L_X = L_Y + 8 * 80, L_H = L_X + 80, L_END = L_H + 64;
constexpr u32 SPIN_G = 1u << 20, SPIN_L = 1u << 23;

struct Params {
  const u16 *k, *wq, *wg, *wu, *x, *v, *wo, *wd;
  u16* out;
  unsigned char* ws;
};

__device__ __forceinline__ float f_(u32 u) { return __builtin_bit_cast(float, u); }
__device__ __forceinline__ u32 u_(float f) { return __builtin_bit_cast(u32, f); }
__device__ __forceinline__ float dot2(u32 a, u32 b, float c) {
  return __builtin_amdgcn_fdot2_f32_bf16(__builtin_bit_cast(bf16x2, a), __builtin_bit_cast(bf16x2, b), c, false);
}
__device__ __forceinline__ u32 bfbits(float f) { return (u32)__builtin_bit_cast(u16, (__bf16)f); }
__device__ __forceinline__ u32 bfpack(float lo, float hi) { return __builtin_bit_cast(u32, bf16x2{(__bf16)lo, (__bf16)hi}); }
__device__ __forceinline__ float rbf(float f) { return f_(bfbits(f) << 16); }
template <int CTRL> __device__ __forceinline__ float dpp(float x) {
  return f_((u32)__builtin_amdgcn_mov_dpp((int)u_(x), CTRL, 0xf, 0xf, true));
}
constexpr int X1 = 0xB1, X2 = 0x4E, X7 = 0x141, X8 = 0x128;
__device__ __forceinline__ float mrg16(float a, float b) {
  auto r = __builtin_amdgcn_permlane16_swap(u_(a), u_(b), false, false);
  return f_(r[0]) + f_(r[1]);
}
__device__ __forceinline__ float mrg32(float a, float b) {
  auto r = __builtin_amdgcn_permlane32_swap(u_(a), u_(b), false, false);
  return f_(r[0]) + f_(r[1]);
}
template <int CTRL> __device__ __forceinline__ float mrgd(float a, float b, bool bit) {
  const float ta = a + dpp<CTRL>(a), tb = b + dpp<CTRL>(b);
  return bit ? tb : ta;
}
__device__ __forceinline__ float sum8(float x) { x += dpp<X1>(x); x += dpp<X2>(x); x += dpp<X7>(x); return x; }
__device__ __forceinline__ float max8(float x) { x = fmaxf(x, dpp<X1>(x)); x = fmaxf(x, dpp<X2>(x)); x = fmaxf(x, dpp<X7>(x)); return x; }
__device__ __forceinline__ float sumhi(float x) { x += dpp<X8>(x); x = mrg16(x, x); return mrg32(x, x); }
__device__ __forceinline__ float maxhi(float x) {
  x = fmaxf(x, dpp<X8>(x));
  auto r = __builtin_amdgcn_permlane16_swap(u_(x), u_(x), false, false); x = fmaxf(f_(r[0]), f_(r[1]));
  auto t = __builtin_amdgcn_permlane32_swap(u_(x), u_(x), false, false); return fmaxf(f_(t[0]), f_(t[1]));
}
__device__ __forceinline__ float sum64(float x) { return sumhi(sum8(x)); }
__device__ __forceinline__ float max64(float x) { return maxhi(max8(x)); }
__device__ __forceinline__ float add8(const float (&v)[8]) { return ((v[0] + v[1]) + (v[2] + v[3])) + ((v[4] + v[5]) + (v[6] + v[7])); }
__device__ __forceinline__ rsrc_t mk(const void* p, int bytes) { return __builtin_amdgcn_make_buffer_rsrc((void*)p, 0, bytes, 0x00020000); }
__device__ __forceinline__ u32 bperm(int srclane, u32 v) { return (u32)__builtin_amdgcn_ds_bpermute(srclane * 4, (int)v); }
__device__ __forceinline__ u32 now32() { return (u32)__builtin_amdgcn_s_memrealtime(); }
#define LGKM0() asm volatile("s_waitcnt lgkmcnt(0)" ::: "memory")
#define VM0() asm volatile("s_waitcnt vmcnt(0)" ::: "memory")
#define BAR() asm volatile("s_waitcnt lgkmcnt(0)\n\ts_barrier" ::: "memory")
#define VMWAIT(n) asm volatile("s_waitcnt vmcnt(%0)" ::"i"(n))
#define SB() __builtin_amdgcn_sched_barrier(0)
#define NAP() asm volatile("s_sleep 1" ::: "memory")
constexpr int brev5(int n) { return ((n & 1) << 4) | ((n & 2) << 2) | (n & 4) | ((n & 8) >> 2) | ((n & 16) >> 4); }
constexpr int imin(int a, int b) { return a < b ? a : b; }
constexpr int imax(int a, int b) { return a > b ? a : b; }
constexpr int sched(int n) { return TOFF + n * TPACE / 64 - HEAD * TPACE / 64; }

template <int N> struct IC { static constexpr int v = N; };
template <int I, int N, class Fn> __device__ __forceinline__ void sfor(Fn&& f) {
  if constexpr (I < N) { f(IC<I>{}); sfor<I + 1, N>(f); }
}
__device__ __forceinline__ void lds_wait_ge(volatile LAS u32* p, u32 want) {
#pragma unroll 1
  for (u32 i = 0; i < SPIN_L; ++i) { if (*p >= want) break; NAP(); }
  asm volatile("" ::: "memory");
}
__device__ __forceinline__ void lds_signal(LAS u32* p, int lane) {
  LGKM0();
  if (lane == 0) __hip_atomic_fetch_add(p, 1u, __ATOMIC_RELAXED, __HIP_MEMORY_SCOPE_WORKGROUP);
}
__device__ __forceinline__ void snooze(int n) {
#pragma unroll 1
  for (int i = 0; i < n; ++i) __builtin_amdgcn_s_sleep(2);
}
__device__ __forceinline__ u32 mix32(u32 x) {
  x ^= 0x5bd1e995u; x ^= x >> 16; x *= 0x85ebca6bu; x ^= x >> 13; x *= 0xc2b2ae35u; x ^= x >> 16;
  return x;
}
template <int S> __device__ __forceinline__ u64 to_fixed(float y, bool odd) {
  const double d = __builtin_fma((double)(odd ? 0.f : y), (double)(1ull << S), 6755399441055744.0);
  return (__builtin_bit_cast(u64, d) << 12) | (odd ? 65ull : 1ull);
}
template <int S> __device__ __forceinline__ float from_fixed(u64 t) {
  return fmaf((float)(int)(u32)(t >> 32), 4294967296.0f, (float)((u32)t & 0xfffff000u)) * (1.0f / 4096.0f / (float)(1ull << S));
}
__device__ __forceinline__ float sum_granules(unsigned char* ws, int off, int stride, int n, u32 want, u32 tag) {
  float r = 0.f;
#pragma unroll 1
  for (u32 it = 0; it < SPIN_G / 8; ++it) {
    u32 found = 0u;
    r = 0.f;
#pragma unroll 1
    for (int i = 0; i < n; ++i) {
      const u64 x = __hip_atomic_load((gu64*)(ws + off + i * stride), __ATOMIC_RELAXED, __HIP_MEMORY_SCOPE_AGENT);
      if ((u32)(x >> 32) == tag) { ++found; r += f_((u32)x); }
    }
    if (found == want) break;
  }
  return r;
}
__device__ __forceinline__ void axpy6(float (&y)[6], v3u a, u32 hb) {
  const u32 hh = hb << 16;
  y[0] = dot2(a.x, hb, y[0]); y[1] = dot2(a.x, hh, y[1]); y[2] = dot2(a.y, hb, y[2]); y[3] = dot2(a.y, hh, y[3]);
  y[4] = dot2(a.z, hb, y[4]); y[5] = dot2(a.z, hh, y[5]);
}
__device__ __forceinline__ v4f rowmac(v4f d, v4u w, u32 vv, v4u msk) {
  const v4u av = {vv & msk.x, vv & msk.y, vv & msk.z, vv & msk.w};
  return __builtin_amdgcn_mfma_f32_16x16x32_bf16(__builtin_bit_cast(bf16x8, av), __builtin_bit_cast(bf16x8, w), d, 0, 0, 0);
}
__device__ __forceinline__ v4u elem_mask(int s8) {
  const u32 m = 0xffffu << (16 * (s8 & 1));
  return v4u{(s8 >> 1) == 0 ? m : 0u, (s8 >> 1) == 1 ? m : 0u, (s8 >> 1) == 2 ? m : 0u, (s8 >> 1) == 3 ? m : 0u};
}
__device__ __forceinline__ u32 dup_half(u32 pair, u32 sel) { return __builtin_amdgcn_perm(pair, pair, sel); }
template <int N, int STR> __device__ __forceinline__ void gather16(v4u (&q)[N], rsrc_t rs, int off, u32 tag) {
  bool ok = false;
#pragma unroll 1
  for (u32 it = 0; it < SPIN_G; ++it) {
    if (!ok) {
#pragma unroll
      for (int i = 0; i < N; ++i) q[i] = __builtin_amdgcn_raw_buffer_load_b128(rs, off, i * STR, SC1);
      ok = true;
#pragma unroll
      for (int i = 0; i < N; ++i) ok &= (q[i].y == tag) & (q[i].w == tag);
    }
    if (__all(ok)) break;
    asm volatile("s_sleep 4" ::: "memory");
  }
}
template <int INC, int WM> __device__ __forceinline__ void pace(u32& due, int t1, int t2, int t3, LAS u32* la) {
  u32 tmp;
  asm volatile("s_waitcnt vmcnt(%10)\n"
               "1:\n\tds_read_b32 %1, %9 offset:32\n\ts_memrealtime vcc\n\ts_waitcnt lgkmcnt(0)\n\tv_readfirstlane_b32 vcc_hi, %1\n\ts_sub_u32 vcc_lo, vcc_lo, %0\n\t"
               "s_sub_u32 vcc_lo, vcc_lo, vcc_hi\n\ts_cmp_lt_i32 vcc_lo, 0\n\ts_cbranch_scc1 1b\n\t"
               "s_cmp_ge_i32 vcc_lo, %5\n\ts_cbranch_scc1 2f\n\ts_waitcnt vmcnt(%6)\n\t"
               "s_cmp_ge_i32 vcc_lo, %4\n\ts_cbranch_scc1 2f\n\ts_waitcnt vmcnt(%7)\n\t"
               "s_cmp_ge_i32 vcc_lo, %3\n\ts_cbranch_scc1 2f\n\ts_waitcnt vmcnt(%8)\n"
               "2:\n\ts_add_u32 %0, %0, %2\n\tv_mov_b32 %1, %0\n\tds_write_b32 %9, %1"
               : "+s"(due), "=&v"(tmp)
               : "i"(INC), "s"(t1), "s"(t2), "s"(t3), "i"(WM + 1), "i"(WM), "i"(WM - 1), "v"(la), "i"(WM + 2) : "scc", "vcc");
}
template <int INC> __device__ __forceinline__ void pace_clock(u32& due, LAS u32* la) {
  u32 tmp;
  asm volatile("1:\n\tds_read_b32 %1, %3 offset:32\n\ts_memrealtime vcc\n\ts_waitcnt lgkmcnt(0)\n\tv_readfirstlane_b32 vcc_hi, %1\n\ts_sub_u32 vcc_lo, vcc_lo, %0\n\t"
               "s_sub_u32 vcc_lo, vcc_lo, vcc_hi\n\ts_cmp_lt_i32 vcc_lo, 0\n\ts_cbranch_scc1 1b\n\ts_add_u32 %0, %0, %2"
               : "+s"(due), "=&v"(tmp) : "i"(INC), "v"(la) : "scc", "vcc");
}
#define TS(i)
#define TSW(i)

__global__ void __launch_bounds__(NTHR, 3) fused_decode(Params) {
  __shared__ __attribute__((aligned(16))) u32 lds_[L_END + (STAMP ? 32 : 0)];
  u64 pc0, tstart;
  v8q ka; v2q kb;
  asm volatile("s_load_dwordx16 %0, %4, 0x0\n\ts_load_dwordx4 %1, %4, 0x40\n\ts_memrealtime %3\n\ts_getpc_b64 %2"
               : "=&s"(ka), "=&s"(kb), "=&s"(pc0), "=&s"(tstart) : "s"(__builtin_amdgcn_kernarg_segment_ptr()));
  const int tid = threadIdx.x, lane = tid & 63, wave = __builtin_amdgcn_readfirstlane(tid >> 6);
  const int b = blockIdx.x, xg = b & 7, g = xg * 32 + (b >> 3), h = g >> 3, s = g & 7, a = h & 3;
  u32 cpf;
  asm volatile("buffer_load_dword %0, %1, %2, 0 offen" : "=v"(cpf)
               : "v"(tid * 128 + ((b & 24) << 20)), "s"(v4u{(u32)pc0, (u32)(pc0 >> 32) & 0xffffu, (u32)CODE_BYTES, 0x00020000u}));
  const int fo = (g * 8 + wave - NHELP) * (64 * DH * 2), vo = lane * 12;
  asm volatile("s_waitcnt lgkmcnt(0)" : "+s"(ka), "+s"(kb), "+s"(tstart));
  Params p;
  p.k = (const u16*)(GAS const u16*)ka[0]; p.wq = (const u16*)(GAS const u16*)ka[1]; p.wg = (const u16*)(GAS const u16*)ka[2]; p.wu = (const u16*)(GAS const u16*)ka[3];
  p.x = (const u16*)(GAS const u16*)ka[4]; p.v = (const u16*)(GAS const u16*)ka[5]; p.wo = (const u16*)(GAS const u16*)ka[6]; p.wd = (const u16*)(GAS const u16*)ka[7];
  p.out = (u16*)(GAS u16*)kb[0]; p.ws = (unsigned char*)(GAS unsigned char*)kb[1];
  v3u kv[32];
  int wave2 = wave;
  if (wave >= NHELP) {
    static_assert(HEAD == 2, "two loads here");
    asm volatile("buffer_load_dwordx3 %0, %2, %3, %4 offen nt\n\tbuffer_load_dwordx3 %1, %2, %3, %4 offen offset:768 nt" : "=&v"(kv[0]), "=&v"(kv[1])
                 : "v"(vo), "s"(v4u{(u32)ka[0], (u32)(ka[0] >> 32) & 0xffffu, (u32)(NH * SK * DH * 2), 0x00020000u}), "s"(fo));
    if (wave == NTHR / 64 - 1) {
      constexpr auto st = [](int span) { return (span - 4) / 28 * 4; };
      u32 drop;
      asm volatile("s_load_dword %0, %1, %2\n\ts_load_dword %0, %3, 0x0\n\ts_load_dword %0, %4, %5\n\ts_load_dword %0, %6, %7\n\ts_load_dword %0, %8, %7\n\t"
                   "s_load_dword %0, %9, %10\n\ts_load_dword %0, %11, %12\n\ts_load_dword %0, %13, %14\n\ts_load_dword %0, %15, 0x0\n\ts_waitcnt lgkmcnt(0)"
                   : "=&s"(drop) : "s"(p.wq), "s"(s * st(D * HD * 2)), "s"(p.x), "s"(p.ws), "s"(512 + (s & 1) * (WS_END - 576)), "s"(p.wg),
                     "s"(s * st(D * FF * 2)), "s"(p.wu), "s"(p.v), "s"(xg * (4 * SK * DH * 2) + s * st(4 * SK * DH * 2)), "s"(p.wd),
                     "s"(xg * (192 * D * 2) + s * st(192 * D * 2)), "s"(p.wo), "s"(xg * (4 * DH * D * 2) + s * st(4 * DH * D * 2)), "s"(p.out));
    }
  }
  asm volatile("" : "+s"(wave2));
  SB();
  LAS u32* L = (LAS u32*)lds_;
  LAS float* Lf = (LAS float*)lds_;
  const int hf = lane >> 5, c = lane & 31, pg = lane >> 3, s8 = lane & 7, r4 = lane >> 4, c16 = lane & 15;
  const rsrc_t rws = mk(p.ws, WS_END);

  if (__builtin_expect(wave2 >= NHELP, 0)) {
    const int sw = wave2 - NHELP;
    const rsrc_t rk = mk((const char*)p.k + fo, 64 * DH * 2), rv = mk((const char*)p.v + fo, 64 * DH * 2);
    const rsrc_t ro = mk(p.wo, HD * D * 2), rd = mk(p.wd, FF * D * 2);
    LAS u32* la = L + L_DUE + sw;
    u32 due = (u32)tstart + TOFF + sw * (TPACE / 64) / 8;
    u32 q0, q1, q2, pb, hk0, hk1;
    float t1[16], sc, fw, acc[6] = {0.f, 0.f, 0.f, 0.f, 0.f, 0.f};
    v2u gbase, te;
    v4u wa[6], wd0, wd1, msk;
    gu64 *pao, *pay;
    u64 ngb;
    int oo, od;
    LAS float *lacw, *lacc, *lyw, *lyr;
    const int col = sw * 64 + lane;
    BAR();
    sfor<HEAD, 68>([&](auto ic) {
      constexpr int n = decltype(ic)::v;
      if constexpr (n == 10) gbase = __builtin_amdgcn_raw_buffer_load_b64(rws, lane < 24 ? OFF_AO + (h * 24 + 3 * sw + pg) * 256 + s8 * 8 : OOB, 0, SC1 | NT);
      if constexpr (n == 32) {
        TS(3);
        lds_wait_ge(L + L_CNT + 1, 1u);
        q0 = L[L_QS + 3 * c]; q1 = L[L_QS + 3 * c + 1]; q2 = L[L_QS + 3 * c + 2];
      }
      if constexpr (n >= 32 && n < 40) {
#pragma unroll
        for (int i = 2 * (n - 32); i < 2 * (n - 31); ++i) {
          const float s0 = dot2(kv[2 * i].z, q2, dot2(kv[2 * i].y, q1, dot2(kv[2 * i].x, q0, 0.f)));
          const float s1 = dot2(kv[2 * i + 1].z, q2, dot2(kv[2 * i + 1].y, q1, dot2(kv[2 * i + 1].x, q0, 0.f)));
          t1[i] = mrg16(s0, s1);
          asm volatile("" : "+v"(t1[i]));
        }
      }
      if constexpr (n == 40) {
        float t2[8], t3[4];
#pragma unroll
        for (int i = 0; i < 8; ++i) t2[i] = mrgd<X8>(t1[2 * i], t1[2 * i + 1], (lane & 8) != 0);
#pragma unroll
        for (int i = 0; i < 4; ++i) t3[i] = mrgd<X7>(t2[2 * i], t2[2 * i + 1], (lane & 4) != 0);
        const float t40 = mrgd<X2>(t3[0], t3[1], (lane & 2) != 0), t41 = mrgd<X2>(t3[2], t3[3], (lane & 2) != 0);
        sc = rbf(rbf(mrgd<X1>(t40, t41, (lane & 1) != 0)) * 0.07216878364870322f);
        asm volatile("" : "+v"(sc));
      }
      if constexpr (n == 41) {
        const float mw = max64(sc);
        const float e = __expf(sc - mw);
        const float lw = sum64(e);
        pb = bfbits(e);
        if (lane == 0) { Lf[L_ML + 2 * sw] = mw; Lf[L_ML + 2 * sw + 1] = lw; }
        asm volatile("" : "+v"(gbase) : "v"(cpf));
        lds_signal(L + L_CNT + 2, lane);
      }
      constexpr auto cons = [](int m) { return imax(0, imin(2 * (m - 41), m - 32 - (m < 64 ? WIN : m == 64 ? 5 : 3))); };
      constexpr int c1 = cons(n), c0 = cons(n - 1);
      sfor<c0, c1>([&](auto jc) {
        constexpr int i = decltype(jc)::v;
        axpy6(acc, kv[i], (u32)__builtin_amdgcn_ds_swizzle((int)pb, brev5(i) << 5));
      });
      if constexpr (c1 > c0) asm volatile("" : "+v"(acc[0]), "+v"(acc[1]), "+v"(acc[2]), "+v"(acc[3]), "+v"(acc[4]), "+v"(acc[5]));
      SB();
      if constexpr (n == 58) due += 7 * (TPACE / 64) / 16 - sw * (TPACE / 64) / 8;
      if constexpr (n < PH2) pace<sched(n + 1) - sched(n), 3>(due, 30, 90, 150, la);
      else if constexpr (n < 64) pace<n == 63 ? TTAIL : sched(n + 1) - sched(n), 4>(due, 20, 60, 100, la);
      else if constexpr (n < 67) pace_clock<TTAIL>(due, la);
      if constexpr (n < 32) kv[n] = __builtin_amdgcn_raw_buffer_load_b96(rk, vo, n * 768, NT);
      else if constexpr (n < 64) kv[n - 32] = __builtin_amdgcn_raw_buffer_load_b96(rv, vo, (n - 32) * 768, NT);
      else if constexpr (n < 67) wa[n - 64] = __builtin_amdgcn_raw_buffer_load_b128(ro, oo, (n - 64) * 4 * D * 2, 0);
      if constexpr (n == 64) wd0 = __builtin_amdgcn_raw_buffer_load_b128(rd, od, 0, 0);
      if constexpr (n == 65) wd1 = __builtin_amdgcn_raw_buffer_load_b128(rd, sw < 4 ? od : OOB, 32 * D * 2, 0);
      SB();
      if constexpr (n == 47) {
        const int oc = 80 * s + col;
        msk = elem_mask(s8);
        pao = (gu64*)(p.ws + OFF_AO + (h * 24 + 3 * sw + pg) * 256 + s8 * 8);
        pay = (gu64*)(p.ws + OFF_AY + (oc >> 3) * 256 + (oc & 7) * 8);
        ngb = 0ull - __builtin_bit_cast(u64, gbase);
        oo = c16 < 10 ? (DH * h + 24 * sw + r4) * (D * 2) + s * 160 + c16 * 16 : OOB;
        od = c16 < 10 ? (48 * h + 4 * sw + r4) * (D * 2) + s * 160 + c16 * 16 : OOB;
        lacw = Lf + L_ACC + sw * 192 + 6 * c + hf; lacc = Lf + L_ACC + 24 * sw + lane;
        lyw = Lf + L_Y + sw * 80 + 8 * c16 + 4 * r4; lyr = Lf + L_Y + col;
        te = *(LAS v2u*)(L + L_E);
        asm volatile("" : "+v"(msk), "+v"(pao), "+v"(pay), "+v"(ngb), "+v"(oo), "+v"(od), "+v"(lacw), "+v"(lacc), "+v"(lyw), "+v"(lyr), "+v"(te));
      }
      if constexpr (n == 60) {
        lds_wait_ge(L + L_CNT + 3, 2u);
        fw = Lf[L_FW + sw]; hk0 = L[L_H + 4 * sw + r4]; hk1 = L[L_H + 32 + 4 * sw + r4];
        asm volatile("" : "+v"(fw), "+v"(hk0), "+v"(hk1));
      }
    });

#pragma unroll
    for (int i = 0; i < 3; ++i) lacw[2 * i] = mrg32(acc[2 * i], acc[2 * i + 1]) * fw;
    SB();
    wa[3] = __builtin_amdgcn_raw_buffer_load_b128(ro, oo, 12 * D * 2, 0);
    TS(14);
    BAR();
    TS(5);
    u32 ob;
    {
      float v[8];
#pragma unroll
      for (int w = 0; w < 8; ++w) v[w] = lacc[w * 192];
      const float o = add8(v);
      const u64 add = to_fixed<36>(o, !(__builtin_fabsf(o) < 2048.f));
      const v4u ro4 = {(u32)(size_t)p.wo, (u32)((size_t)p.wo >> 32) & 0xffffu, (u32)(HD * D * 2), 0x00020000u};
      u64 t, ra, tmp;
      u32 cnt = SPIN_G;
      asm volatile("s_mov_b64 exec, %[m]\n\tglobal_atomic_add_x2 %[ra], %[pa], %[add], off sc0\n\ts_mov_b64 exec, -1\n\t"
                   "buffer_load_dwordx4 %[w4], %[oo], %[ro], %[s4] offen\n\tbuffer_load_dwordx4 %[w5], %[oo], %[ro], %[s5] offen\n\ts_mov_b64 exec, %[m]\n\t"
                   "v_lshl_add_u64 %[tmp], %[add], 0, %[ng]\n\ts_waitcnt vmcnt(2)\n\tv_lshl_add_u64 %[t], %[ra], 0, %[tmp]\n"
                   "1:\n\tv_lshlrev_b64 %[tmp], 58, %[t]\n\tv_cmp_ne_u64 vcc, %[c8], %[tmp]\n\ts_cbranch_vccz 2f\n\t"
                   "global_atomic_add_x2 %[ra], %[pa], %[z], off sc0\n\ts_waitcnt vmcnt(0)\n\tv_lshl_add_u64 %[t], %[ra], 0, %[ng]\n\t"
                   "s_sub_u32 %[cnt], %[cnt], 1\n\ts_cbranch_scc0 1b\n"
                   "2:\n\ts_mov_b64 exec, -1\n\ts_waitcnt vmcnt(0)"
                   : [ra] "=&v"(ra), [t] "=&v"(t), [tmp] "=&v"(tmp), [cnt] "+s"(cnt), [w4] "=&v"(wa[4]), [w5] "=&v"(wa[5])
                   : [pa] "v"(pao), [add] "v"(add), [z] "v"(0ull), [ng] "v"(ngb), [c8] "s"(8ull << 58), [m] "s"(0xffffffull),
                     [oo] "v"(oo), [ro] "s"(ro4), [s4] "s"(16 * D * 2), [s5] "s"(20 * D * 2)
                   : "vcc", "scc", "memory");
      float of = from_fixed<36>(t);
      if (__builtin_expect((lane < 24) & (FORCE_FB | (((u32)t & 0xfc0u) != 0u) | (__builtin_fabsf(of) < 0x1p-22f)), 0)) {
        const int row = 24 * sw + lane;
        __builtin_amdgcn_raw_buffer_store_b64(v2u{u_(o), te.x}, rws, OFF_G1 + g * G1_STR + row * 8, 0, SC1);
        of = sum_granules(p.ws, OFF_G1 + h * 8 * G1_STR + row * 8, G1_STR, 8, 8u, te.x);
      }
      ob = bfpack(of, of);
    }
    TS(6);
    {
      u32 pv[6];
#pragma unroll
      for (int i = 0; i < 6; ++i) pv[i] = bperm(4 * i + r4, ob);
      SB();
      v4f d = {0.f, 0.f, 0.f, 0.f};
      d = rowmac(d, wd0, hk0, msk); d = rowmac(d, wd1, hk1, msk);
#pragma unroll
      for (int i = 0; i < 6; ++i) d = rowmac(d, wa[i], pv[i], msk);
      if (hf == 0 && c16 < 10) *(LAS v4f*)lyw = d;
    }
    BAR();
    TS(7);
    if (col < 80) {
      float v[8];
#pragma unroll
      for (int w = 0; w < 8; ++w) v[w] = lyr[w * 80];
      const float xv = lyr[8 * 80], yy = add8(v), ay = __builtin_fabsf(yy);
      const bool odd = FORCE_FB ? ((h + col) & 3) == 0 : (!(ay < 4096.f)) | ((ay < 0x1p-23f) & (yy != 0.f));
      const u64 mine = to_fixed<34>(yy, odd);
      const u64 t = __hip_atomic_fetch_add(pay, mine, __ATOMIC_RELAXED, __HIP_MEMORY_SCOPE_AGENT) + mine;
      if (__builtin_expect(odd, 0)) __builtin_amdgcn_raw_buffer_store_b64(v2u{u_(yy), te.x}, rws, OFF_GA + (s * 32 + h) * 640 + col * 8, 0, SC1);
      if (((u32)t & 63u) == 32u) {
        float av = from_fixed<34>(t);
        const u32 nodd = ((u32)t >> 6) & 63u;
        if (__builtin_expect(nodd != 0u, 0)) av += sum_granules(p.ws, OFF_GA + s * 32 * 640 + col * 8, 640, 32, nodd, te.x);
        __builtin_amdgcn_raw_buffer_store_b16((u16)bfbits(xv + av), mk(p.out, D * 2), col * 2, s * 160, 0);
        if (s == 0 && col == 0) __builtin_amdgcn_raw_buffer_store_b32(te.y, rws, OFF_E, 0, 0);
      }
    }
  } else {
    const int hw = wave2;
    __builtin_amdgcn_s_setprio(3);
    const bool gu = s >= 6;
    const int j = 2 * a + (s & 1);
    const int row0 = gu ? 160 * (j & 3) + 40 * hw : 320 * (s & 1) + 80 * hw;
    const int pitch = gu ? FF * 2 : HD * 2;
    const int wo0 = (row0 + pg) * pitch + (gu ? xg * 384 : h * 384 + (s >> 1) * 128) + s8 * 16;
    const u16* wbase = gu ? (j < 4 ? p.wg : p.wu) : p.wq;
    const rsrc_t rw = mk(wbase, D * pitch), rw2 = mk(wbase, gu ? D * pitch : 0);
    const int st8 = 8 * pitch, st5 = gu ? 128 : 40 * pitch;
    v4u wv[15];
#pragma unroll
    for (int k = 0; k < 3; ++k) wv[k] = __builtin_amdgcn_raw_buffer_load_b128(rw, wo0, k * st8, NT);
    SB();
    const rsrc_t rx = mk(p.x, D * 2);
    const v4u xa = __builtin_amdgcn_raw_buffer_load_b128(rx, lane * 16, 0, 0);
    const u32 xb = __builtin_amdgcn_raw_buffer_load_b32(rx, 1024 + lane * 4, 0, 0);
    const u32 xr = __builtin_amdgcn_raw_buffer_load_b32(rx, row0 * 2 + lane * 4, 0, 0);
    u32 eraw = __builtin_amdgcn_raw_buffer_load_b32(rws, OFF_E, 0, SC1);
    SB();
    if (hw == 0 && lane < 32) L[L_CNT + lane] = (lane & 24) == L_DUE ? (u32)tstart + TOFF : 0u;
#pragma unroll
    for (int k = 3; k < 15; ++k) wv[k] = __builtin_amdgcn_raw_buffer_load_b128(k < 10 ? rw : rw2, wo0, (k / 5) * st5 + (k % 5) * st8, NT);
    SB(); BAR(); SB();
    float xv[10];
    xv[0] = f_(xa.x << 16); xv[1] = f_(xa.x & 0xffff0000u); xv[2] = f_(xa.y << 16); xv[3] = f_(xa.y & 0xffff0000u);
    xv[4] = f_(xa.z << 16); xv[5] = f_(xa.z & 0xffff0000u); xv[6] = f_(xa.w << 16); xv[7] = f_(xa.w & 0xffff0000u);
    xv[8] = f_(xb << 16); xv[9] = f_(xb & 0xffff0000u);
    float sm = 0.f;
#pragma unroll
    for (int i = 0; i < 10; ++i) sm += xv[i];
    const float mean = sum64(sm) * (1.0f / D);
    float sq = 0.f;
#pragma unroll
    for (int i = 0; i < 10; ++i) { const float d = xv[i] - mean; sq = fmaf(d, d, sq); }
    const float var = sum64(sq) * (1.0f / D);
    const float den = rbf(sqrtf(rbf(rbf(var) + 1e-5f))), mb = rbf(mean);
    const u32 xn = bfpack(rbf(f_(xr << 16) - mb) / den, rbf(f_(xr & 0xffff0000u) - mb) / den);
    asm volatile("" : "+v"(eraw) : "v"(cpf));
    const u32 tag = mix32(eraw);
    if (hw == 1 && h == 0 && lane < 40)
      __builtin_amdgcn_raw_buffer_store_b128(v4u{0u, 0u, 0u, 0u}, rws, OFF_AY + (s * 10 + (lane >> 2)) * 256 + (lane & 3) * 16, 0, SC1);
    {
      const v4u msk = elem_mask(s8);
      const u32 sel = (pg & 1) ? 0x03020302u : 0x01000100u;
      const int sl5 = gu ? 0 : 20;
      v4f d[3] = {v4f{0.f, 0.f, 0.f, 0.f}, v4f{0.f, 0.f, 0.f, 0.f}, v4f{0.f, 0.f, 0.f, 0.f}};
#pragma unroll
      for (int k = 0; k < 15; ++k) d[k / 5] = rowmac(d[k / 5], wv[k], dup_half(bperm((k / 5) * sl5 + (k % 5) * 4 + r4, xn), sel), msk);
      if ((c16 >> 3) == hf) {
#pragma unroll
        for (int i = 0; i < 3; ++i) *(LAS v4f*)(Lf + L_HP + (hw * 3 + i) * 128 + hf * 64 + 8 * s8 + 4 * (r4 & 1)) = d[i];
      }
    }
    lds_signal(L + L_CNT + 0, lane);
    lds_wait_ge(L + L_CNT + 0, 4u);
    if (hw < (gu ? 3 : 1)) {
      float v = 0.f;
#pragma unroll 1
      for (int t = hw; t <= (gu ? hw : 1); ++t) {
        float e[8];
#pragma unroll
        for (int i = 0; i < 8; ++i) e[i] = Lf[L_HP + ((i >> 1) * 3 + t) * 128 + (i & 1) * 64 + lane];
        v += add8(e);
      }
      __builtin_amdgcn_raw_buffer_store_b64(v2u{u_(v), tag}, rws, OFF_G0 + g * G0_STR + hw * 512 + lane * 8, 0, SC1);
    }
    TS(1);
    if (hw == 0) {
      snooze(28);
      v4u q4[3];
      gather16<3, 2 * G0_STR>(q4, rws, OFF_G0 + (h * 8 + hf) * G0_STR + c * 16, tag);
#pragma unroll
      for (int i = 0; i < 3; ++i) {
        const u32 pk = bfpack(mrg32(f_(q4[i].x), f_(q4[i].x)), mrg32(f_(q4[i].z), f_(q4[i].z)));
        if (hf == 0) L[L_QS + 32 * i + c] = pk;
      }
      if (lane == 0) *(LAS v2u*)(L + L_E) = v2u{tag, eraw + 1u};
      lds_signal(L + L_CNT + 1, lane);
      TS(2); TSW(1); TSW(2);
      lds_wait_ge(L + L_CNT + 2, 8u);
      const float mw = Lf[L_ML + 2 * s8], lw = Lf[L_ML + 2 * s8 + 1];
      const float mg = max8(mw), lg = sum8(__expf(mw - mg) * lw);
      if (lane == 0) __builtin_amdgcn_raw_buffer_store_b128(v4u{u_(mg), tag, u_(lg), tag}, rws, OFF_G1 + g * G1_STR + 1536, 0, SC1);
      snooze(24);
      v4u ml[1];
      gather16<1, 0>(ml, rws, OFF_G1 + (h * 8 + pg) * G1_STR + 1536, tag);
      const float M = maxhi(f_(ml[0].x));
      const float Ls = sumhi(__expf(f_(ml[0].x) - M) * f_(ml[0].z));
      if (lane < 8) Lf[L_FW + lane] = __expf(mw - M) / Ls;
      lds_signal(L + L_CNT + 3, lane);
      TS(10); TSW(10);
    } else if (hw == 1) {
      if (h == 0) {
        VM0();
        if (lane == 0) __builtin_amdgcn_raw_buffer_store_b128(v4u{0u, tag, 0u, tag}, rws, OFF_GF + s * 128, 0, SC1);
      }
    } else if (hw == 2) {
      const u32 t0 = (u32)tstart;
      volatile LAS u32* dues = L + L_DUE + s8;
      int shift = 0;
      float bs = 0.f, dr = 0.f;
      bool fin = false;
      auto report = [&](int r) {
        const int late = max(0, (int)(now32() - shift - *dues));
        const int e = shift + (int)(sum8((float)late) * 0.125f);
        if (b < NREP && lane == 0) __builtin_amdgcn_raw_buffer_store_b64(v2u{t0 + e, tag}, rws, OFF_GT + r * 512 + b * 8, 0, SC1);
      };
      while ((int)(now32() - t0) < CA1) NAP();
      report(0);
      u32 tb = t0, tr = now32(), tn = tr + CR1;
      auto run_until = [&](u32 t) {
#pragma unroll 1
        for (u32 i = 0; i < SPIN_L; ++i) {
          const u32 n_ = now32();
          shift = (int)(bs + dr * (float)(int)(n_ - tr));
          if (lane < 8) L[L_SH + lane] = shift;
          fin = __any((int)(*dues - t0) >= sched(61));
          if (fin || (int)(n_ - t) >= 0) break;
          NAP();
        }
      };
#pragma unroll 1
      for (int r = 0; r < RN; ++r) {
        u32 tcur = tr;
        if (r) {
          tcur = tb + CT2 + (r - 1) * CTD;
          run_until(tcur);
          if ((int)(now32() - tcur) < CRD / 2 && !fin) report(r);
          tn = tcur + CRD;
        }
        run_until(tn);
        if (__any((int)(*dues - t0) >= sched(55))) break;
        const v2u gt = __builtin_amdgcn_raw_buffer_load_b64(rws, OFF_GT + r * 512 + (lane & (NREP - 1)) * 8, 0, SC1);
        const bool ok = gt.y == tag;
        const float cnt = sum64(ok ? 1.f : 0.f), sum = sum64(ok ? fminf(fmaxf((float)(int)(gt.x - t0), -300.f), 800.f) : 0.f);
        if (cnt > 0.f) {
          const float nb = sum / cnt - (r == 0 ? 25.f : r == 1 ? 20.f : 15.f);
          if (r) dr = fminf(fmaxf((nb - bs) * (r == 1 ? 0.5f / (CT2 - CA1) : 0.75f / (float)(int)(tcur - tr)), 0.f), 0.2f);
          bs = nb; tr = tcur;
        }
        if (r == 0) tb = t0 + __builtin_amdgcn_readfirstlane((int)bs);
      }
      run_until(now32() + 4000u);
    } else {
      const u32 xp = __builtin_amdgcn_raw_buffer_load_b32(rx, 160 * s + lane * 4, 0, 0);
      snooze(32);
      v4u g4[3];
      gather16<3, 128>(g4, rws, OFF_G0 + ((4 * xg + (pg >> 1)) * 8 + 6 + (pg & 1)) * G0_STR + 384 * a + s8 * 16, tag);
#pragma unroll
      for (int i = 0; i < 3; ++i) {
        u32 hv[2];
#pragma unroll
        for (int e = 0; e < 2; ++e) {
          float t = f_(e ? g4[i].z : g4[i].x);
          t += dpp<X8>(t); t = mrg16(t, t);
          auto r = __builtin_amdgcn_permlane32_swap(u_(t), u_(t), false, false);
          const float gg = rbf(f_(r[0])), uu = rbf(f_(r[1]));
          const float hh = rbf(1.0f / (1.0f + __expf(-gg))) * uu;
          hv[e] = bfpack(hh, hh);
        }
        if (lane < 8) *(LAS v2u*)(L + L_H + 16 * i + 2 * lane) = v2u{hv[0], hv[1]};
      }
      if (lane < 16) L[L_H + 48 + lane] = 0u;
      if (lane < 40) *(LAS v2u*)(L + L_X + 2 * lane) = v2u{xp << 16, xp & 0xffff0000u};
      v4u f4[1];
      gather16<1, 0>(f4, rws, OFF_GF + s * 128, tag);
      lds_signal(L + L_CNT + 3, lane);
      TS(9); TSW(9);
    }
  }
}

extern "C" void kernel_launch(void* const* d_in, const int* in_sizes, int n_in, void* d_out, int out_size, void* d_ws, size_t ws_size,
                              hipStream_t stream) {
  Params p;
  p.x = (const u16*)d_in[0]; p.k = (const u16*)d_in[1]; p.v = (const u16*)d_in[2]; p.wq = (const u16*)d_in[3]; p.wo = (const u16*)d_in[4];
  p.wg = (const u16*)d_in[6]; p.wu = (const u16*)d_in[7]; p.wd = (const u16*)d_in[8];
  p.out = (u16*)d_out; p.ws = (unsigned char*)d_ws;
  hipLaunchKernelGGL(fused_decode, dim3(256), dim3(NTHR), 0, stream, p);
}
```

```cpp
#include <hip/hip_runtime.h>
#include <hip/hip_bf16.h>

typedef unsigned u32;
typedef unsigned short u16;
typedef unsigned long long u64;
typedef u32 v2u __attribute__((ext_vector_type(2)));
typedef u32 v3u __attribute__((ext_vector_type(3)));
typedef u32 v4u __attribute__((ext_vector_type(4)));
typedef float v4f __attribute__((ext_vector_type(4)));
typedef u64 v8q __attribute__((ext_vector_type(8)));
typedef u64 v2q __attribute__((ext_vector_type(2)));
typedef __bf16 bf16x2 __attribute__((ext_vector_type(2)));
typedef __bf16 bf16x8 __attribute__((ext_vector_type(8)));
#define LAS __attribute__((address_space(3)))
#define GAS __attribute__((address_space(1)))
typedef GAS u64 gu64;
typedef __amdgpu_buffer_rsrc_t rsrc_t;

constexpr int D = 640, NH = 32, SK = 4096, DH = 192, FF = 1536, HD = NH * DH;
constexpr int NSTREAM = 8, NHELP = 4, NTHR = 64 * (NSTREAM + NHELP);
constexpr int SC1 = 16, NT = 2, OOB = 0x7ff00000;
constexpr int CODE_BYTES = 20480;
constexpr int HEAD = 2;
constexpr int TOFF = 140, TPACE = 1380;
constexpr int PH2 = 24;
constexpr int WIN = 7, TTAIL = 14;
constexpr int RN = 5, NREP = 32;
constexpr int CA1 = 375, CR1 = 150, CT2 = 695, CTD = 200, CRD = 60;
constexpr int OFF_E = 0;
constexpr int OFF_GF = 1024;
constexpr int OFF_GT = 2048;
constexpr int OFF_G0 = 8192, G0_STR = 1536;
constexpr int OFF_G1 = OFF_G0 + 256 * G0_STR, G1_STR = 1664;
constexpr int OFF_GA = OFF_G1 + 256 * G1_STR;
constexpr int OFF_AO = OFF_GA + 256 * 640;
constexpr int OFF_AY = OFF_AO + 32 * 24 * 256;
constexpr int WS_END = OFF_AY + 80 * 256;
constexpr int L_CNT = 0, L_T0 = 15, L_DUE = 16, L_SH = 24;
constexpr int L_HP = 32, L_QS = L_HP + 4 * 3 * 128, L_E = L_QS + 96, L_ACC = L_E + 4, L_ML = L_ACC + 8 * 192 + 64, L_FW = L_ML + 16,
              L_Y = L_FW + 8, L_X = L_Y + 8 * 80, L_H = L_X + 80, L_END = L_H + 64;
constexpr u32 SPIN_G = 1u << 20, SPIN_L = 1u << 23;

struct Params {
  const u16 *k, *wq, *wg, *wu, *x, *v, *wo, *wd;
  u16* out;
  unsigned char* ws;
};

__device__ __forceinline__ float f_(u32 u) { return __builtin_bit_cast(float, u); }
__device__ __forceinline__ u32 u_(float f) { return __builtin_bit_cast(u32, f); }
__device__ __forceinline__ float dot2(u32 a, u32 b, float c) {
  return __builtin_amdgcn_fdot2_f32_bf16(__builtin_bit_cast(bf16x2, a), __builtin_bit_cast(bf16x2, b), c, false);
}
__device__ __forceinline__ u32 bfbits(float f) { return (u32)__builtin_bit_cast(u16, (__bf16)f); }
__device__ __forceinline__ u32 bfpack(float lo, float hi) { return __builtin_bit_cast(u32, bf16x2{(__bf16)lo, (__bf16)hi}); }
__device__ __forceinline__ float rbf(float f) { return f_(bfbits(f) << 16); }
template <int CTRL> __device__ __forceinline__ float dpp(float x) {
  return f_((u32)__builtin_amdgcn_mov_dpp((int)u_(x), CTRL, 0xf, 0xf, true));
}
constexpr int X1 = 0xB1, X2 = 0x4E, X7 = 0x141, X8 = 0x128;
__device__ __forceinline__ float mrg16(float a, float b) {
  auto r = __builtin_amdgcn_permlane16_swap(u_(a), u_(b), false, false);
  return f_(r[0]) + f_(r[1]);
}
__device__ __forceinline__ float mrg32(float a, float b) {
  auto r = __builtin_amdgcn_permlane32_swap(u_(a), u_(b), false, false);
  return f_(r[0]) + f_(r[1]);
}
template <int CTRL> __device__ __forceinline__ float mrgd(float a, float b, bool bit) {
  const float ta = a + dpp<CTRL>(a), tb = b + dpp<CTRL>(b);
  return bit ? tb : ta;
}
__device__ __forceinline__ float sum8(float x) { x += dpp<X1>(x); x += dpp<X2>(x); x += dpp<X7>(x); return x; }
__device__ __forceinline__ float max8(float x) { x = fmaxf(x, dpp<X1>(x)); x = fmaxf(x, dpp<X2>(x)); x = fmaxf(x, dpp<X7>(x)); return x; }
__device__ __forceinline__ float sumhi(float x) { x += dpp<X8>(x); x = mrg16(x, x); return mrg32(x, x); }
__device__ __forceinline__ float maxhi(float x) {
  x = fmaxf(x, dpp<X8>(x));
  auto r = __builtin_amdgcn_permlane16_swap(u_(x), u_(x), false, false); x = fmaxf(f_(r[0]), f_(r[1]));
  auto t = __builtin_amdgcn_permlane32_swap(u_(x), u_(x), false, false); return fmaxf(f_(t[0]), f_(t[1]));
}
__device__ __forceinline__ float sum64(float x) { return sumhi(sum8(x)); }
__device__ __forceinline__ float max64(float x) { return maxhi(max8(x)); }
__device__ __forceinline__ float add8(const float (&v)[8]) { return ((v[0] + v[1]) + (v[2] + v[3])) + ((v[4] + v[5]) + (v[6] + v[7])); }
__device__ __forceinline__ rsrc_t mk(const void* p, int bytes) { return __builtin_amdgcn_make_buffer_rsrc((void*)p, 0, bytes, 0x00020000); }
__device__ __forceinline__ u32 bperm(int srclane, u32 v) { return (u32)__builtin_amdgcn_ds_bpermute(srclane * 4, (int)v); }
__device__ __forceinline__ u32 now32() { return (u32)__builtin_amdgcn_s_memrealtime(); }
#define LGKM0() asm volatile("s_waitcnt lgkmcnt(0)" ::: "memory")
#define VM0() asm volatile("s_waitcnt vmcnt(0)" ::: "memory")
#define BAR() asm volatile("s_waitcnt lgkmcnt(0)\n\ts_barrier" ::: "memory")
#define SB() __builtin_amdgcn_sched_barrier(0)
#define NAP() asm volatile("s_sleep 1" ::: "memory")
constexpr int brev5(int n) { return ((n & 1) << 4) | ((n & 2) << 2) | (n & 4) | ((n & 8) >> 2) | ((n & 16) >> 4); }
constexpr int imin(int a, int b) { return a < b ? a : b; }
constexpr int imax(int a, int b) { return a > b ? a : b; }
constexpr int sched(int n) { return TOFF + n * TPACE / 64 - HEAD * TPACE / 64; }

template <int N> struct IC { static constexpr int v = N; };
template <int I, int N, class Fn> __device__ __forceinline__ void sfor(Fn&& f) {
  if constexpr (I < N) { f(IC<I>{}); sfor<I + 1, N>(f); }
}
__device__ __forceinline__ void lds_wait_ge(volatile LAS u32* p, u32 want) {
#pragma unroll 1
  for (u32 i = 0; i < SPIN_L; ++i) { if (*p >= want) break; NAP(); }
  asm volatile("" ::: "memory");
}
__device__ __forceinline__ void lds_signal(LAS u32* p, int lane) {
  LGKM0();
  if (lane == 0) __hip_atomic_fetch_add(p, 1u, __ATOMIC_RELAXED, __HIP_MEMORY_SCOPE_WORKGROUP);
}
__device__ __forceinline__ void snooze(int n) {
#pragma unroll 1
  for (int i = 0; i < n; ++i) __builtin_amdgcn_s_sleep(2);
}
__device__ __forceinline__ u32 mix32(u32 x) {
  x ^= 0x5bd1e995u; x ^= x >> 16; x *= 0x85ebca6bu; x ^= x >> 13; x *= 0xc2b2ae35u; x ^= x >> 16;
  return x;
}
template <int S> __device__ __forceinline__ u64 to_fixed(float y, bool odd) {
  const double d = __builtin_fma((double)(odd ? 0.f : y), (double)(1ull << S), 6755399441055744.0);
  return (__builtin_bit_cast(u64, d) << 12) | (odd ? 65ull : 1ull);
}
template <int S> __device__ __forceinline__ float from_fixed(u64 t) {
  return fmaf((float)(int)(u32)(t >> 32), 4294967296.0f, (float)((u32)t & 0xfffff000u)) * (1.0f / 4096.0f / (float)(1ull << S));
}
__device__ __forceinline__ float sum_granules(unsigned char* ws, int off, int stride, int n, u32 want, u32 tag) {
  float r = 0.f;
#pragma unroll 1
  for (u32 it = 0; it < SPIN_G / 8; ++it) {
    u32 found = 0u;
    r = 0.f;
#pragma unroll 1
    for (int i = 0; i < n; ++i) {
      const u64 x = __hip_atomic_load((gu64*)(ws + off + i * stride), __ATOMIC_RELAXED, __HIP_MEMORY_SCOPE_AGENT);
      if ((u32)(x >> 32) == tag) { ++found; r += f_((u32)x); }
    }
    if (found == want) break;
  }
  return r;
}
__device__ __forceinline__ void axpy6(float (&y)[6], v3u a, u32 hb) {
  const u32 hh = hb << 16;
  y[0] = dot2(a.x, hb, y[0]); y[1] = dot2(a.x, hh, y[1]); y[2] = dot2(a.y, hb, y[2]); y[3] = dot2(a.y, hh, y[3]);
  y[4] = dot2(a.z, hb, y[4]); y[5] = dot2(a.z, hh, y[5]);
}
__device__ __forceinline__ v4f rowmac(v4f d, v4u w, u32 vv, v4u msk) {
  const v4u av = {vv & msk.x, vv & msk.y, vv & msk.z, vv & msk.w};
  return __builtin_amdgcn_mfma_f32_16x16x32_bf16(__builtin_bit_cast(bf16x8, av), __builtin_bit_cast(bf16x8, w), d, 0, 0, 0);
}
__device__ __forceinline__ v4u elem_mask(int s8) {
  const u32 m = 0xffffu << (16 * (s8 & 1));
  return v4u{(s8 >> 1) == 0 ? m : 0u, (s8 >> 1) == 1 ? m : 0u, (s8 >> 1) == 2 ? m : 0u, (s8 >> 1) == 3 ? m : 0u};
}
__device__ __forceinline__ u32 dup_half(u32 pair, u32 sel) { return __builtin_amdgcn_perm(pair, pair, sel); }
template <int N, int STR> __device__ __forceinline__ void gather16(v4u (&q)[N], rsrc_t rs, int off, u32 tag) {
  bool ok = false;
#pragma unroll 1
  for (u32 it = 0; it < SPIN_G; ++it) {
    if (!ok) {
#pragma unroll
      for (int i = 0; i < N; ++i) q[i] = __builtin_amdgcn_raw_buffer_load_b128(rs, off, i * STR, SC1);
      ok = true;
#pragma unroll
      for (int i = 0; i < N; ++i) ok &= (q[i].y == tag) & (q[i].w == tag);
    }
    if (__all(ok)) break;
    asm volatile("s_sleep 4" ::: "memory");
  }
}
template <int INC, int WM> __device__ __forceinline__ void pace(u32& due, int t1, int t2, int t3, LAS u32* la) {
  u32 tmp;
  asm volatile("s_waitcnt vmcnt(%10)\n"
               "1:\n\tds_read_b32 %1, %9 offset:32\n\ts_memrealtime vcc\n\ts_waitcnt lgkmcnt(0)\n\tv_readfirstlane_b32 vcc_hi, %1\n\ts_sub_u32 vcc_lo, vcc_lo, %0\n\t"
               "s_sub_u32 vcc_lo, vcc_lo, vcc_hi\n\ts_cmp_lt_i32 vcc_lo, 0\n\ts_cbranch_scc1 1b\n\t"
               "s_cmp_ge_i32 vcc_lo, %5\n\ts_cbranch_scc1 2f\n\ts_waitcnt vmcnt(%6)\n\t"
               "s_cmp_ge_i32 vcc_lo, %4\n\ts_cbranch_scc1 2f\n\ts_waitcnt vmcnt(%7)\n\t"
               "s_cmp_ge_i32 vcc_lo, %3\n\ts_cbranch_scc1 2f\n\ts_waitcnt vmcnt(%8)\n"
               "2:\n\ts_add_u32 %0, %0, %2\n\tv_mov_b32 %1, %0\n\tds_write_b32 %9, %1"
               : "+s"(due), "=&v"(tmp)
               : "i"(INC), "s"(t1), "s"(t2), "s"(t3), "i"(WM + 1), "i"(WM), "i"(WM - 1), "v"(la), "i"(WM + 2) : "scc", "vcc");
}
template <int INC> __device__ __forceinline__ void pace_clock(u32& due, LAS u32* la) {
  u32 tmp;
  asm volatile("1:\n\tds_read_b32 %1, %3 offset:32\n\ts_memrealtime vcc\n\ts_waitcnt lgkmcnt(0)\n\tv_readfirstlane_b32 vcc_hi, %1\n\ts_sub_u32 vcc_lo, vcc_lo, %0\n\t"
               "s_sub_u32 vcc_lo, vcc_lo, vcc_hi\n\ts_cmp_lt_i32 vcc_lo, 0\n\ts_cbranch_scc1 1b\n\ts_add_u32 %0, %0, %2"
               : "+s"(due), "=&v"(tmp) : "i"(INC), "v"(la) : "scc", "vcc");
}

__global__ void __launch_bounds__(NTHR, 3) fused_decode(Params) {
  __shared__ __attribute__((aligned(16))) u32 lds_[L_END];
  u64 pc0;
  v8q ka; v2q kb;
  asm volatile("s_load_dwordx16 %0, %3, 0x0\n\ts_load_dwordx4 %1, %3, 0x40\n\ts_getpc_b64 %2"
               : "=&s"(ka), "=&s"(kb), "=&s"(pc0) : "s"(__builtin_amdgcn_kernarg_segment_ptr()));
  const int tid = threadIdx.x, lane = tid & 63, wave = __builtin_amdgcn_readfirstlane(tid >> 6);
  const int b = blockIdx.x, xg = b & 7, g = xg * 32 + (b >> 3), h = g >> 3, s = g & 7, a = h & 3;
  u32 cpf;
  asm volatile("buffer_load_dword %0, %1, %2, 0 offen" : "=v"(cpf)
               : "v"(tid * 128 + ((b & 24) << 20)), "s"(v4u{(u32)pc0, (u32)(pc0 >> 32) & 0xffffu, (u32)CODE_BYTES, 0x00020000u}));
  const int fo = (g * 8 + wave - NHELP) * (64 * DH * 2), vo = lane * 12;
  asm volatile("s_waitcnt lgkmcnt(0)" : "+s"(ka), "+s"(kb));
  Params p;
  p.k = (const u16*)(GAS const u16*)ka[0]; p.wq = (const u16*)(GAS const u16*)ka[1]; p.wg = (const u16*)(GAS const u16*)ka[2]; p.wu = (const u16*)(GAS const u16*)ka[3];
  p.x = (const u16*)(GAS const u16*)ka[4]; p.v = (const u16*)(GAS const u16*)ka[5]; p.wo = (const u16*)(GAS const u16*)ka[6]; p.wd = (const u16*)(GAS const u16*)ka[7];
  p.out = (u16*)(GAS u16*)kb[0]; p.ws = (unsigned char*)(GAS unsigned char*)kb[1];
  v3u kv[32];
  int wave2 = wave;
  if (wave >= NHELP) {
    static_assert(HEAD == 2, "two loads here");
    asm volatile("buffer_load_dwordx3 %0, %2, %3, %4 offen nt\n\tbuffer_load_dwordx3 %1, %2, %3, %4 offen offset:768 nt" : "=&v"(kv[0]), "=&v"(kv[1])
                 : "v"(vo), "s"(v4u{(u32)ka[0], (u32)(ka[0] >> 32) & 0xffffu, (u32)(NH * SK * DH * 2), 0x00020000u}), "s"(fo));
    if (wave == NTHR / 64 - 1) {
      constexpr auto st = [](int span) { return (span - 4) / 28 * 4; };
      u32 drop;
      asm volatile("s_load_dword %0, %1, %2\n\ts_load_dword %0, %3, 0x0\n\ts_load_dword %0, %4, %5\n\ts_load_dword %0, %6, %7\n\ts_load_dword %0, %8, %7\n\t"
                   "s_load_dword %0, %9, %10\n\ts_load_dword %0, %11, %12\n\ts_load_dword %0, %13, %14\n\ts_load_dword %0, %15, 0x0\n\ts_waitcnt lgkmcnt(0)"
                   : "=&s"(drop) : "s"(p.wq), "s"(s * st(D * HD * 2)), "s"(p.x), "s"(p.ws), "s"(512 + (s & 1) * (WS_END - 576)), "s"(p.wg),
                     "s"(s * st(D * FF * 2)), "s"(p.wu), "s"(p.v), "s"(xg * (4 * SK * DH * 2) + s * st(4 * SK * DH * 2)), "s"(p.wd),
                     "s"(xg * (192 * D * 2) + s * st(192 * D * 2)), "s"(p.wo), "s"(xg * (4 * DH * D * 2) + s * st(4 * DH * D * 2)), "s"(p.out));
    }
  }
  asm volatile("" : "+s"(wave2));
  SB();
  LAS u32* L = (LAS u32*)lds_;
  LAS float* Lf = (LAS float*)lds_;
  const int hf = lane >> 5, c = lane & 31, pg = lane >> 3, s8 = lane & 7, r4 = lane >> 4, c16 = lane & 15;
  const rsrc_t rws = mk(p.ws, WS_END);

  if (__builtin_expect(wave2 >= NHELP, 0)) {
    const int sw = wave2 - NHELP;
    const rsrc_t rk = mk((const char*)p.k + fo, 64 * DH * 2), rv = mk((const char*)p.v + fo, 64 * DH * 2);
    const rsrc_t ro = mk(p.wo, HD * D * 2), rd = mk(p.wd, FF * D * 2);
    LAS u32* la = L + L_DUE + sw;
    u32 q0, q1, q2, pb, hk0, hk1;
    float t1[16], sc, fw, acc[6] = {0.f, 0.f, 0.f, 0.f, 0.f, 0.f};
    v2u gbase, te;
    v4u wa[6], wd0, wd1, msk;
    gu64 *pao, *pay;
    u64 ngb;
    int oo, od;
    LAS float *lacw, *lacc, *lyw, *lyr;
    const int col = sw * 64 + lane;
    BAR();
    u32 due = __builtin_amdgcn_readfirstlane(L[L_T0]) + sw * (TPACE / 64) / 8;
    sfor<HEAD, 68>([&](auto ic) {
      constexpr int n = decltype(ic)::v;
      if constexpr (n == 10) gbase = __builtin_amdgcn_raw_buffer_load_b64(rws, lane < 24 ? OFF_AO + (h * 24 + 3 * sw + pg) * 256 + s8 * 8 : OOB, 0, SC1 | NT);
      if constexpr (n == 32) {
        lds_wait_ge(L + L_CNT + 1, 1u);
        q0 = L[L_QS + 3 * c]; q1 = L[L_QS + 3 * c + 1]; q2 = L[L_QS + 3 * c + 2];
      }
      if constexpr (n >= 32 && n < 40) {
#pragma unroll
        for (int i = 2 * (n - 32); i < 2 * (n - 31); ++i) {
          const float s0 = dot2(kv[2 * i].z, q2, dot2(kv[2 * i].y, q1, dot2(kv[2 * i].x, q0, 0.f)));
          const float s1 = dot2(kv[2 * i + 1].z, q2, dot2(kv[2 * i + 1].y, q1, dot2(kv[2 * i + 1].x, q0, 0.f)));
          t1[i] = mrg16(s0, s1);
          asm volatile("" : "+v"(t1[i]));
        }
      }
      if constexpr (n == 40) {
        float t2[8], t3[4];
#pragma unroll
        for (int i = 0; i < 8; ++i) t2[i] = mrgd<X8>(t1[2 * i], t1[2 * i + 1], (lane & 8) != 0);
#pragma unroll
        for (int i = 0; i < 4; ++i) t3[i] = mrgd<X7>(t2[2 * i], t2[2 * i + 1], (lane & 4) != 0);
        const float t40 = mrgd<X2>(t3[0], t3[1], (lane & 2) != 0), t41 = mrgd<X2>(t3[2], t3[3], (lane & 2) != 0);
        sc = rbf(rbf(mrgd<X1>(t40, t41, (lane & 1) != 0)) * 0.07216878364870322f);
        asm volatile("" : "+v"(sc));
      }
      if constexpr (n == 41) {
        const float mw = max64(sc);
        const float e = __expf(sc - mw);
        const float lw = sum64(e);
        pb = bfbits(e);
        if (lane == 0) { Lf[L_ML + 2 * sw] = mw; Lf[L_ML + 2 * sw + 1] = lw; }
        asm volatile("" : "+v"(gbase) : "v"(cpf));
        lds_signal(L + L_CNT + 2, lane);
      }
      constexpr auto cons = [](int m) { return imax(0, imin(2 * (m - 41), m - 32 - (m < 64 ? WIN : m == 64 ? 5 : 3))); };
      constexpr int c1 = cons(n), c0 = cons(n - 1);
      sfor<c0, c1>([&](auto jc) {
        constexpr int i = decltype(jc)::v;
        axpy6(acc, kv[i], (u32)__builtin_amdgcn_ds_swizzle((int)pb, brev5(i) << 5));
      });
      if constexpr (c1 > c0) asm volatile("" : "+v"(acc[0]), "+v"(acc[1]), "+v"(acc[2]), "+v"(acc[3]), "+v"(acc[4]), "+v"(acc[5]));
      SB();
      if constexpr (n == 58) due += 7 * (TPACE / 64) / 16 - sw * (TPACE / 64) / 8;
      if constexpr (n < PH2) pace<sched(n + 1) - sched(n), 3>(due, 30, 90, 150, la);
      else if constexpr (n < 64) pace<n == 63 ? TTAIL : sched(n + 1) - sched(n), 4>(due, 20, 60, 100, la);
      else if constexpr (n < 67) pace_clock<TTAIL>(due, la);
      if constexpr (n < 32) kv[n] = __builtin_amdgcn_raw_buffer_load_b96(rk, vo, n * 768, NT);
      else if constexpr (n < 64) kv[n - 32] = __builtin_amdgcn_raw_buffer_load_b96(rv, vo, (n - 32) * 768, NT);
      else if constexpr (n < 67) wa[n - 64] = __builtin_amdgcn_raw_buffer_load_b128(ro, oo, (n - 64) * 4 * D * 2, 0);
      if constexpr (n == 64) wd0 = __builtin_amdgcn_raw_buffer_load_b128(rd, od, 0, 0);
      if constexpr (n == 65) wd1 = __builtin_amdgcn_raw_buffer_load_b128(rd, sw < 4 ? od : OOB, 32 * D * 2, 0);
      SB();
      if constexpr (n == 47) {
        const int oc = 80 * s + col;
        msk = elem_mask(s8);
        pao = (gu64*)(p.ws + OFF_AO + (h * 24 + 3 * sw + pg) * 256 + s8 * 8);
        pay = (gu64*)(p.ws + OFF_AY + (oc >> 3) * 256 + (oc & 7) * 8);
        ngb = 0ull - __builtin_bit_cast(u64, gbase);
        oo = c16 < 10 ? (DH * h + 24 * sw + r4) * (D * 2) + s * 160 + c16 * 16 : OOB;
        od = c16 < 10 ? (48 * h + 4 * sw + r4) * (D * 2) + s * 160 + c16 * 16 : OOB;
        lacw = Lf + L_ACC + sw * 192 + 6 * c + hf; lacc = Lf + L_ACC + 24 * sw + lane;
        lyw = Lf + L_Y + sw * 80 + 8 * c16 + 4 * r4; lyr = Lf + L_Y + col;
        te = *(LAS v2u*)(L + L_E);
        asm volatile("" : "+v"(msk), "+v"(pao), "+v"(pay), "+v"(ngb), "+v"(oo), "+v"(od), "+v"(lacw), "+v"(lacc), "+v"(lyw), "+v"(lyr), "+v"(te));
      }
      if constexpr (n == 60) {
        lds_wait_ge(L + L_CNT + 3, 2u);
        fw = Lf[L_FW + sw]; hk0 = L[L_H + 4 * sw + r4]; hk1 = L[L_H + 32 + 4 * sw + r4];
        asm volatile("" : "+v"(fw), "+v"(hk0), "+v"(hk1));
      }
    });

#pragma unroll
    for (int i = 0; i < 3; ++i) lacw[2 * i] = mrg32(acc[2 * i], acc[2 * i + 1]) * fw;
    SB();
    wa[3] = __builtin_amdgcn_raw_buffer_load_b128(ro, oo, 12 * D * 2, 0);
    BAR();
    u32 ob;
    {
      float v[8];
#pragma unroll
      for (int w = 0; w < 8; ++w) v[w] = lacc[w * 192];
      const float o = add8(v);
      const u64 add = to_fixed<36>(o, !(__builtin_fabsf(o) < 2048.f));
      const v4u ro4 = {(u32)(size_t)p.wo, (u32)((size_t)p.wo >> 32) & 0xffffu, (u32)(HD * D * 2), 0x00020000u};
      u64 t, ra, tmp;
      u32 cnt = SPIN_G;
      asm volatile("s_mov_b64 exec, %[m]\n\tglobal_atomic_add_x2 %[ra], %[pa], %[add], off sc0\n\ts_mov_b64 exec, -1\n\t"
                   "buffer_load_dwordx4 %[w4], %[oo], %[ro], %[s4] offen\n\tbuffer_load_dwordx4 %[w5], %[oo], %[ro], %[s5] offen\n\ts_mov_b64 exec, %[m]\n\t"
                   "v_lshl_add_u64 %[tmp], %[add], 0, %[ng]\n\ts_waitcnt vmcnt(2)\n\tv_lshl_add_u64 %[t], %[ra], 0, %[tmp]\n"
                   "1:\n\tv_lshlrev_b64 %[tmp], 58, %[t]\n\tv_cmp_ne_u64 vcc, %[c8], %[tmp]\n\ts_cbranch_vccz 2f\n\t"
                   "global_atomic_add_x2 %[ra], %[pa], %[z], off sc0\n\ts_waitcnt vmcnt(0)\n\tv_lshl_add_u64 %[t], %[ra], 0, %[ng]\n\t"
                   "s_sub_u32 %[cnt], %[cnt], 1\n\ts_cbranch_scc0 1b\n"
                   "2:\n\ts_mov_b64 exec, -1\n\ts_waitcnt vmcnt(0)"
                   : [ra] "=&v"(ra), [t] "=&v"(t), [tmp] "=&v"(tmp), [cnt] "+s"(cnt), [w4] "=&v"(wa[4]), [w5] "=&v"(wa[5])
                   : [pa] "v"(pao), [add] "v"(add), [z] "v"(0ull), [ng] "v"(ngb), [c8] "s"(8ull << 58), [m] "s"(0xffffffull),
                     [oo] "v"(oo), [ro] "s"(ro4), [s4] "s"(16 * D * 2), [s5] "s"(20 * D * 2)
                   : "vcc", "scc", "memory");
      float of = from_fixed<36>(t);
      if (__builtin_expect((lane < 24) & ((((u32)t & 0xfc0u) != 0u) | (__builtin_fabsf(of) < 0x1p-22f)), 0)) {
        const int row = 24 * sw + lane;
        __builtin_amdgcn_raw_buffer_store_b64(v2u{u_(o), te.x}, rws, OFF_G1 + g * G1_STR + row * 8, 0, SC1);
        of = sum_granules(p.ws, OFF_G1 + h * 8 * G1_STR + row * 8, G1_STR, 8, 8u, te.x);
      }
      ob = bfpack(of, of);
    }
    {
      u32 pv[6];
#pragma unroll
      for (int i = 0; i < 6; ++i) pv[i] = bperm(4 * i + r4, ob);
      SB();
      v4f d = {0.f, 0.f, 0.f, 0.f};
      d = rowmac(d, wd0, hk0, msk); d = rowmac(d, wd1, hk1, msk);
#pragma unroll
      for (int i = 0; i < 6; ++i) d = rowmac(d, wa[i], pv[i], msk);
      if (hf == 0 && c16 < 10) *(LAS v4f*)lyw = d;
    }
    BAR();
    if (col < 80) {
      float v[8];
#pragma unroll
      for (int w = 0; w < 8; ++w) v[w] = lyr[w * 80];
      const float xv = lyr[8 * 80], yy = add8(v), ay = __builtin_fabsf(yy);
      const bool odd = (!(ay < 4096.f)) | ((ay < 0x1p-23f) & (yy != 0.f));
      const u64 mine = to_fixed<34>(yy, odd);
      const u64 t = __hip_atomic_fetch_add(pay, mine, __ATOMIC_RELAXED, __HIP_MEMORY_SCOPE_AGENT) + mine;
      if (__builtin_expect(odd, 0)) __builtin_amdgcn_raw_buffer_store_b64(v2u{u_(yy), te.x}, rws, OFF_GA + (s * 32 + h) * 640 + col * 8, 0, SC1);
      if (((u32)t & 63u) == 32u) {
        float av = from_fixed<34>(t);
        const u32 nodd = ((u32)t >> 6) & 63u;
        if (__builtin_expect(nodd != 0u, 0)) av += sum_granules(p.ws, OFF_GA + s * 32 * 640 + col * 8, 640, 32, nodd, te.x);
        __builtin_amdgcn_raw_buffer_store_b16((u16)bfbits(xv + av), mk(p.out, D * 2), col * 2, s * 160, 0);
        if (s == 0 && col == 0) __builtin_amdgcn_raw_buffer_store_b32(te.y, rws, OFF_E, 0, 0);
      }
    }
  } else {
    const int hw = wave2;
    u64 tbase = __builtin_amdgcn_s_memrealtime();
    SB();
    __builtin_amdgcn_s_setprio(3);
    const bool gu = s >= 6;
    const int j = 2 * a + (s & 1);
    const int row0 = gu ? 160 * (j & 3) + 40 * hw : 320 * (s & 1) + 80 * hw;
    const int pitch = gu ? FF * 2 : HD * 2;
    const int wo0 = (row0 + pg) * pitch + (gu ? xg * 384 : h * 384 + (s >> 1) * 128) + s8 * 16;
    const u16* wbase = gu ? (j < 4 ? p.wg : p.wu) : p.wq;
    const rsrc_t rw = mk(wbase, D * pitch), rw2 = mk(wbase, gu ? D * pitch : 0);
    const int st8 = 8 * pitch, st5 = gu ? 128 : 40 * pitch;
    v4u wv[15];
#pragma unroll
    for (int k = 0; k < 3; ++k) wv[k] = __builtin_amdgcn_raw_buffer_load_b128(rw, wo0, k * st8, NT);
    SB();
    const rsrc_t rx = mk(p.x, D * 2);
    const v4u xa = __builtin_amdgcn_raw_buffer_load_b128(rx, lane * 16, 0, 0);
    const u32 xb = __builtin_amdgcn_raw_buffer_load_b32(rx, 1024 + lane * 4, 0, 0);
    const u32 xr = __builtin_amdgcn_raw_buffer_load_b32(rx, row0 * 2 + lane * 4, 0, 0);
    u32 eraw = __builtin_amdgcn_raw_buffer_load_b32(rws, OFF_E, 0, SC1);
    SB();
#pragma unroll
    for (int k = 3; k < 15; ++k) wv[k] = __builtin_amdgcn_raw_buffer_load_b128(k < 10 ? rw : rw2, wo0, (k / 5) * st5 + (k % 5) * st8, NT);
    SB();
    asm volatile("" : "+s"(tbase));
    if (hw == 0 && lane < 32) L[L_CNT + lane] = (lane & 24) == L_DUE || lane == L_T0 ? (u32)tbase + TOFF : 0u;
    BAR(); SB();
    float xv[10];
    xv[0] = f_(xa.x << 16); xv[1] = f_(xa.x & 0xffff0000u); xv[2] = f_(xa.y << 16); xv[3] = f_(xa.y & 0xffff0000u);
    xv[4] = f_(xa.z << 16); xv[5] = f_(xa.z & 0xffff0000u); xv[6] = f_(xa.w << 16); xv[7] = f_(xa.w & 0xffff0000u);
    xv[8] = f_(xb << 16); xv[9] = f_(xb & 0xffff0000u);
    float sm = 0.f;
#pragma unroll
    for (int i = 0; i < 10; ++i) sm += xv[i];
    const float mean = sum64(sm) * (1.0f / D);
    float sq = 0.f;
#pragma unroll
    for (int i = 0; i < 10; ++i) { const float d = xv[i] - mean; sq = fmaf(d, d, sq); }
    const float var = sum64(sq) * (1.0f / D);
    const float den = rbf(sqrtf(rbf(rbf(var) + 1e-5f))), mb = rbf(mean);
    const u32 xn = bfpack(rbf(f_(xr << 16) - mb) / den, rbf(f_(xr & 0xffff0000u) - mb) / den);
    asm volatile("" : "+v"(eraw) : "v"(cpf));
    const u32 tag = mix32(eraw);
    if (hw == 1 && h == 0 && lane < 40)
      __builtin_amdgcn_raw_buffer_store_b128(v4u{0u, 0u, 0u, 0u}, rws, OFF_AY + (s * 10 + (lane >> 2)) * 256 + (lane & 3) * 16, 0, SC1);
    {
      const v4u msk = elem_mask(s8);
      const u32 sel = (pg & 1) ? 0x03020302u : 0x01000100u;
      const int sl5 = gu ? 0 : 20;
      v4f d[3] = {v4f{0.f, 0.f, 0.f, 0.f}, v4f{0.f, 0.f, 0.f, 0.f}, v4f{0.f, 0.f, 0.f, 0.f}};
#pragma unroll
      for (int k = 0; k < 15; ++k) d[k / 5] = rowmac(d[k / 5], wv[k], dup_half(bperm((k / 5) * sl5 + (k % 5) * 4 + r4, xn), sel), msk);
      if ((c16 >> 3) == hf) {
#pragma unroll
        for (int i = 0; i < 3; ++i) *(LAS v4f*)(Lf + L_HP + (hw * 3 + i) * 128 + hf * 64 + 8 * s8 + 4 * (r4 & 1)) = d[i];
      }
    }
    lds_signal(L + L_CNT + 0, lane);
    lds_wait_ge(L + L_CNT + 0, 4u);
    if (hw < (gu ? 3 : 1)) {
      float v = 0.f;
#pragma unroll 1
      for (int t = hw; t <= (gu ? hw : 1); ++t) {
        float e[8];
#pragma unroll
        for (int i = 0; i < 8; ++i) e[i] = Lf[L_HP + ((i >> 1) * 3 + t) * 128 + (i & 1) * 64 + lane];
        v += add8(e);
      }
      __builtin_amdgcn_raw_buffer_store_b64(v2u{u_(v), tag}, rws, OFF_G0 + g * G0_STR + hw * 512 + lane * 8, 0, SC1);
    }
    if (hw == 0) {
      snooze(28);
      v4u q4[3];
      gather16<3, 2 * G0_STR>(q4, rws, OFF_G0 + (h * 8 + hf) * G0_STR + c * 16, tag);
#pragma unroll
      for (int i = 0; i < 3; ++i) {
        const u32 pk = bfpack(mrg32(f_(q4[i].x), f_(q4[i].x)), mrg32(f_(q4[i].z), f_(q4[i].z)));
        if (hf == 0) L[L_QS + 32 * i + c] = pk;
      }
      if (lane == 0) *(LAS v2u*)(L + L_E) = v2u{tag, eraw + 1u};
      lds_signal(L + L_CNT + 1, lane);
      lds_wait_ge(L + L_CNT + 2, 8u);
      const float mw = Lf[L_ML + 2 * s8], lw = Lf[L_ML + 2 * s8 + 1];
      const float mg = max8(mw), lg = sum8(__expf(mw - mg) * lw);
      if (lane == 0) __builtin_amdgcn_raw_buffer_store_b128(v4u{u_(mg), tag, u_(lg), tag}, rws, OFF_G1 + g * G1_STR + 1536, 0, SC1);
      snooze(24);
      v4u ml[1];
      gather16<1, 0>(ml, rws, OFF_G1 + (h * 8 + pg) * G1_STR + 1536, tag);
      const float M = maxhi(f_(ml[0].x));
      const float Ls = sumhi(__expf(f_(ml[0].x) - M) * f_(ml[0].z));
      if (lane < 8) Lf[L_FW + lane] = __expf(mw - M) / Ls;
      lds_signal(L + L_CNT + 3, lane);
    } else if (hw == 1) {
      if (h == 0) {
        VM0();
        if (lane == 0) __builtin_amdgcn_raw_buffer_store_b128(v4u{0u, tag, 0u, tag}, rws, OFF_GF + s * 128, 0, SC1);
      }
    } else if (hw == 2) {
      const u32 t0 = __builtin_amdgcn_readfirstlane(L[L_T0]) - TOFF;
      volatile LAS u32* dues = L + L_DUE + s8;
      int shift = 0;
      float bs = 0.f, dr = 0.f;
      bool fin = false;
      auto report = [&](int r) {
        const int late = max(0, (int)(now32() - shift - *dues));
        const int e = shift + (int)(sum8((float)late) * 0.125f);
        if (b < NREP && lane == 0) __builtin_amdgcn_raw_buffer_store_b64(v2u{t0 + e, tag}, rws, OFF_GT + r * 512 + b * 8, 0, SC1);
      };
      while ((int)(now32() - t0) < CA1) NAP();
      report(0);
      u32 tb = t0, tr = now32(), tn = tr + CR1;
      auto run_until = [&](u32 t) {
#pragma unroll 1
        for (u32 i = 0; i < SPIN_L; ++i) {
          const u32 n_ = now32();
          shift = (int)(bs + dr * (float)(int)(n_ - tr));
          if (lane < 8) L[L_SH + lane] = shift;
          fin = __any((int)(*dues - t0) >= sched(61));
          if (fin || (int)(n_ - t) >= 0) break;
          NAP();
        }
      };
#pragma unroll 1
      for (int r = 0; r < RN; ++r) {
        u32 tcur = tr;
        if (r) {
          tcur = tb + CT2 + (r - 1) * CTD;
          run_until(tcur);
          if ((int)(now32() - tcur) < CRD / 2 && !fin) report(r);
          tn = tcur + CRD;
        }
        run_until(tn);
        if (__any((int)(*dues - t0) >= sched(55))) break;
        const v2u gt = __builtin_amdgcn_raw_buffer_load_b64(rws, OFF_GT + r * 512 + (lane & (NREP - 1)) * 8, 0, SC1);
        const bool ok = gt.y == tag;
        const float cnt = sum64(ok ? 1.f : 0.f), sum = sum64(ok ? fminf(fmaxf((float)(int)(gt.x - t0), -300.f), 800.f) : 0.f);
        if (cnt > 0.f) {
          const float nb = sum / cnt - (r == 0 ? 25.f : r == 1 ? 20.f : 15.f);
          if (r) dr = fminf(fmaxf((nb - bs) * (r == 1 ? 0.5f / (CT2 - CA1) : 0.75f / (float)(int)(tcur - tr)), 0.f), 0.2f);
          bs = nb; tr = tcur;
        }
        if (r == 0) tb = t0 + __builtin_amdgcn_readfirstlane((int)bs);
      }
      run_until(now32() + 4000u);
    } else {
      const u32 xp = __builtin_amdgcn_raw_buffer_load_b32(rx, 160 * s + lane * 4, 0, 0);
      snooze(32);
      v4u g4[3];
      gather16<3, 128>(g4, rws, OFF_G0 + ((4 * xg + (pg >> 1)) * 8 + 6 + (pg & 1)) * G0_STR + 384 * a + s8 * 16, tag);
#pragma unroll
      for (int i = 0; i < 3; ++i) {
        u32 hv[2];
#pragma unroll
        for (int e = 0; e < 2; ++e) {
          float t = f_(e ? g4[i].z : g4[i].x);
          t += dpp<X8>(t); t = mrg16(t, t);
          auto r = __builtin_amdgcn_permlane32_swap(u_(t), u_(t), false, false);
          const float gg = rbf(f_(r[0])), uu = rbf(f_(r[1]));
          const float hh = rbf(1.0f / (1.0f + __expf(-gg))) * uu;
          hv[e] = bfpack(hh, hh);
        }
        if (lane < 8) *(LAS v2u*)(L + L_H + 16 * i + 2 * lane) = v2u{hv[0], hv[1]};
      }
      if (lane < 16) L[L_H + 48 + lane] = 0u;
      if (lane < 40) *(LAS v2u*)(L + L_X + 2 * lane) = v2u{xp << 16, xp & 0xffff0000u};
      v4u f4[1];
      gather16<1, 0>(f4, rws, OFF_GF + s * 128, tag);
      lds_signal(L + L_CNT + 3, lane);
    }
  }
}

extern "C" void kernel_launch(void* const* d_in, const int* in_sizes, int n_in, void* d_out, int out_size, void* d_ws, size_t ws_size,
                              hipStream_t stream) {
  Params p;
  p.x = (const u16*)d_in[0]; p.k = (const u16*)d_in[1]; p.v = (const u16*)d_in[2]; p.wq = (const u16*)d_in[3]; p.wo = (const u16*)d_in[4];
  p.wg = (const u16*)d_in[6]; p.wu = (const u16*)d_in[7]; p.wd = (const u16*)d_in[8];
  p.out = (u16*)d_out; p.ws = (unsigned char*)d_ws;
  hipLaunchKernelGGL(fused_decode, dim3(256), dim3(NTHR), 0, stream, p);
}
```
